```python
import torch

def _softmax(x, axis=-1):
    return torch.softmax(x, dim=axis)

def _gelu(x):
    return torch.nn.functional.gelu(x, approximate='tanh')

def _silu(x):
    return torch.nn.functional.silu(x)

def _sigmoid(x):
    return torch.sigmoid(x)

def _layernorm(x, axis=-1, eps=1e-05):
    m = x.mean(dim=axis, keepdim=True)
    v = x.var(dim=axis, keepdim=True, unbiased=False)
    return (x - m) / torch.sqrt(v + eps)

def _scatter_add(tgt, idx, src, axis=0):
    return tgt.clone().index_add_(axis, idx, src)

def _segment_sum(src, seg, n):
    out = torch.zeros((n,) + src.shape[1:], dtype=src.dtype)
    return out.index_add_(0, seg, src)

def _pad(x, pw):
    flat = tuple((v for pair in reversed(pw) for v in pair))
    return torch.nn.functional.pad(x, flat)
kernel_name = 'standalone__tree_speculative_verify_tree_attention_over_candidate_token_tree__sampling'

def setup_inputs():

    _w = lambda a: (lambda t: t.to(torch.bfloat16) if t.is_floating_point() else t)(torch.as_tensor(a))
    B, Hq, Hkv, T, C, D = (8, 32, 32, 16, 4096, 128)
    tree_mask = torch.zeros((B, T, T), dtype=float)
    for b in range(B):
        parent = torch.zeros(T, dtype=torch.int64)
        for i in range(1, T):
            parent[i] = int(torch.randint(0, i, (1,)).item())
        for i in range(T):
            j = i
            tree_mask[b, i, j] = 1.0
            while j > 0:
                j = parent[j]
                tree_mask[b, i, j] = 1.0
    return {'q': _w((lambda *s: torch.randn(*s, dtype=torch.bfloat16) * 0.8)(B, Hq, T, D)), 'k_cache': _w((lambda *s: torch.randn(*s, dtype=torch.bfloat16) * 0.8)(B, Hkv, C, D)), 'v_cache': _w((lambda *s: torch.randn(*s, dtype=torch.bfloat16) * 0.05)(B, Hkv, C, D)), 'k_tree': _w((lambda *s: torch.randn(*s, dtype=torch.bfloat16) * 0.8)(B, Hkv, T, D)), 'v_tree': _w((lambda *s: torch.randn(*s, dtype=torch.bfloat16) * 0.05)(B, Hkv, T, D)), 'tree_mask': _w(tree_mask)}

def reference(q, k_cache, v_cache, k_tree, v_tree, tree_mask):
    B, Hq, T, D = q.shape
    Hkv, C = (k_cache.shape[1], k_cache.shape[2])
    rep = Hq // Hkv
    kc = (lambda x, n, axis: torch.repeat_interleave(x, n, dim=axis))(k_cache, rep, axis=1)
    vc = (lambda x, n, axis: torch.repeat_interleave(x, n, dim=axis))(v_cache, rep, axis=1)
    kt = (lambda x, n, axis: torch.repeat_interleave(x, n, dim=axis))(k_tree, rep, axis=1)
    vt = (lambda x, n, axis: torch.repeat_interleave(x, n, dim=axis))(v_tree, rep, axis=1)
    k = torch.concatenate([kc, kt], axis=2)
    v = torch.concatenate([vc, vt], axis=2)
    scale = D ** (-0.5)
    scores = torch.einsum('bhqd,bhkd->bhqk', q, k) * scale
    ctx_mask = torch.ones((B, 1, T, C), dtype=scores.dtype)
    tm = tree_mask[:, None, :, :].clone()
    full_mask = torch.concatenate([ctx_mask, tm], axis=-1)
    scores = scores + (1.0 - full_mask) * -1000000000.0
    p = _softmax(scores, axis=-1)
    return torch.einsum('bhqk,bhkd->bhqd', p, v)
```

```cpp
#include <hip/hip_runtime.h>
#include <hip/hip_bf16.h>
#include <stdint.h>

#define NW 4
#define DEPTH 2
#define NTFLAG " nt"
#define INTERLEAVE 1

namespace {

constexpr int B_ = 8, H_ = 32, T_ = 16, C_ = 4096, D_ = 128;
constexpr int NSTEP = C_ / 32;
constexpr int NS = NSTEP / NW;
constexpr int RING = DEPTH * 16384;
constexpr int LDS_Q = NW * RING;
constexpr int LDS_KT = LDS_Q + 4096;
constexpr int LDS_VT = LDS_KT + 4096;
constexpr int LDS_MASK = LDS_VT + 4096;
constexpr int LDS_TOTAL = LDS_MASK + 1024;
constexpr int MROW = 528;
static_assert(LDS_TOTAL <= 163840, "LDS");
static_assert(16 * MROW + 128 <= RING, "merge region");

typedef __attribute__((ext_vector_type(8))) __bf16 bf16x8;
typedef __attribute__((ext_vector_type(4))) __bf16 bf16x4;
typedef __attribute__((ext_vector_type(4))) short s16x4;
typedef __attribute__((ext_vector_type(8))) short s16x8;
typedef __attribute__((ext_vector_type(4))) float f32x4;
typedef __attribute__((ext_vector_type(4))) unsigned u32x4;
typedef __attribute__((ext_vector_type(2))) unsigned u32x2;
#define LAS __attribute__((address_space(3)))
#define SB() __builtin_amdgcn_sched_barrier(0)

__device__ __forceinline__ unsigned cvtpk(float lo, float hi) {
  typedef __attribute__((ext_vector_type(2))) __bf16 bf16x2;
  typedef __attribute__((ext_vector_type(2))) float f32x2;
  f32x2 v = {lo, hi};
  bf16x2 r = __builtin_convertvector(v, bf16x2);
  return __builtin_bit_cast(unsigned, r);
}
__device__ __forceinline__ float xg_max(float x) {
  auto s = __builtin_amdgcn_permlane16_swap(__float_as_uint(x), __float_as_uint(x), false, false);
  x = fmaxf(__uint_as_float(s[0]), __uint_as_float(s[1]));
  auto t = __builtin_amdgcn_permlane32_swap(__float_as_uint(x), __float_as_uint(x), false, false);
  return fmaxf(__uint_as_float(t[0]), __uint_as_float(t[1]));
}
__device__ __forceinline__ float xg_sum(float x) {
  auto s = __builtin_amdgcn_permlane16_swap(__float_as_uint(x), __float_as_uint(x), false, false);
  x = __uint_as_float(s[0]) + __uint_as_float(s[1]);
  auto t = __builtin_amdgcn_permlane32_swap(__float_as_uint(x), __float_as_uint(x), false, false);
  return __uint_as_float(t[0]) + __uint_as_float(t[1]);
}

__device__ __forceinline__ void dma_tile(unsigned m0v, unsigned v0, unsigned v1, unsigned v2, unsigned v3,
                                         __amdgpu_buffer_rsrc_t srd, unsigned soff) {
  asm volatile(
      "s_mov_b32 m0, %0\n\t"
      "s_nop 0\n\t"
      "buffer_load_dwordx4 %1, %5, %6 offen" NTFLAG " lds\n\t"
      "buffer_load_dwordx4 %2, %5, %6 offen offset:1024" NTFLAG " lds\n\t"
      "buffer_load_dwordx4 %3, %5, %6 offen offset:2048" NTFLAG " lds\n\t"
      "buffer_load_dwordx4 %4, %5, %6 offen offset:3072" NTFLAG " lds"
      :: "s"(m0v), "v"(v0), "v"(v1), "v"(v2), "v"(v3), "s"(srd), "s"(soff) : "memory");
}
template <int N> __device__ __forceinline__ void wait_vm() { asm volatile("s_waitcnt vmcnt(%0)" :: "i"(N) : "memory"); }
__device__ __forceinline__ void wait_lgkm0() { asm volatile("s_waitcnt lgkmcnt(0)" ::: "memory"); }

struct State {
  bf16x8 qf[4];
  f32x4 acc[8];
  float m, l;
};

constexpr float kScale = 0.088388347648318440f;
constexpr float kL2E = 1.4426950408889634f;
constexpr float kC = kScale * kL2E;

__device__ __forceinline__ bf16x8 lds_b128(const char LAS* p) { return *(const bf16x8 LAS*)p; }
__device__ __forceinline__ s16x4 lds_tr(const char LAS* p) { return __builtin_amdgcn_ds_read_tr16_b64_v4i16((s16x4 LAS*)p); }

}

__global__ __launch_bounds__(NW * 64, 2) void tree_attn_kernel(
    const uint16_t* __restrict__ q, const uint16_t* __restrict__ kc, const uint16_t* __restrict__ vc,
    const uint16_t* __restrict__ kt, const uint16_t* __restrict__ vt, const uint16_t* __restrict__ mask,
    uint16_t* __restrict__ out) {
  extern __shared__ __attribute__((aligned(16))) char smem_[];
  char LAS* smem = (char LAS*)smem_;
  const int tid = threadIdx.x, lane = tid & 63;
  const int w = __builtin_amdgcn_readfirstlane(tid >> 6);
  const int bh = blockIdx.x, b = bh >> 5;
  const unsigned g = lane >> 4, c = lane & 15;
  const unsigned lds0 = (unsigned)(size_t)smem;

  const __amdgpu_buffer_rsrc_t srdK = __builtin_amdgcn_make_buffer_rsrc((void*)(kc + (size_t)bh * C_ * D_), 0, C_ * D_ * 2, 0x00020000);
  const __amdgpu_buffer_rsrc_t srdV = __builtin_amdgcn_make_buffer_rsrc((void*)(vc + (size_t)bh * C_ * D_), 0, C_ * D_ * 2, 0x00020000);

  unsigned vk[4], vv[2];
#pragma unroll
  for (int i = 0; i < 4; ++i) vk[i] = g * 256 + 16 * ((c ^ g) ^ (4 * i));
#pragma unroll
  for (int j = 0; j < 2; ++j) vv[j] = g * 256 + 16 * (c ^ (2 * g) ^ (8 * j));

  const unsigned ring = lds0 + w * RING;
  auto step_off = [&](int s) -> unsigned { return INTERLEAVE ? (unsigned)(s * NW + w) * 8192u : (unsigned)(w * NS + s) * 8192u; };
  auto issueK = [&](int s, int slot) {
    const unsigned o = step_off(s);
    dma_tile(ring + slot * 8192, vk[0], vk[1], vk[2], vk[3], srdK, o);
    dma_tile(ring + slot * 8192 + 4096, vk[0], vk[1], vk[2], vk[3], srdK, o + 4096);
  };
  auto issueV = [&](int s, int slot) {
    const unsigned o = step_off(s);
    dma_tile(ring + DEPTH * 8192 + slot * 8192, vv[0], vv[1], vv[0], vv[1], srdV, o);
    dma_tile(ring + DEPTH * 8192 + slot * 8192 + 4096, vv[0], vv[1], vv[0], vv[1], srdV, o + 4096);
  };

  {
    const size_t tb = (size_t)bh * T_ * D_ * 2;
    constexpr int PPW = (NW >= 4) ? (16 / NW >= 2 ? 16 / NW : 2) : 16 / NW;
#pragma unroll
    for (int j = 0; j < 16 / NW; ++j) {
      const int pi = w * (16 / NW) + j;
      const int kind = pi & 3, rg = pi >> 2;
      const unsigned row = 4 * rg + g;
      const char* src; unsigned dst;
      if (kind == 0)      { src = (const char*)q + tb + row * 256 + 16 * (c ^ row);            dst = LDS_Q + rg * 1024; }
      else if (kind == 1) { src = (const char*)kt + tb + row * 256 + 16 * (c ^ row);           dst = LDS_KT + rg * 1024; }
      else if (kind == 2) { src = (const char*)vt + tb + row * 256 + 16 * (c ^ (2 * (row & 7))); dst = LDS_VT + rg * 1024; }
      else                { src = (const char*)mask + (size_t)b * 512 + (lane & 31) * 16;      dst = LDS_MASK; }
      const unsigned m0v = __builtin_amdgcn_readfirstlane(lds0 + dst);
      asm volatile("s_nop 4\n\ts_mov_b32 m0, %0\n\ts_nop 0\n\tglobal_load_lds_dwordx4 %1, off" :: "s"(m0v), "v"(src) : "memory");
    }
    (void)PPW;
  }
#pragma unroll
  for (int s = 0; s < DEPTH; ++s) { issueK(s, s); issueV(s, s); }

  unsigned ka[4], va[8];
#pragma unroll
  for (int kk = 0; kk < 4; ++kk) ka[kk] = c * 256 + 16 * ((g ^ c) ^ (4 * kk));
  {
    const unsigned qq = c >> 2, p = c & 3, row = 4 * g + qq;
#pragma unroll
    for (int dt = 0; dt < 8; ++dt) va[dt] = row * 256 + 32 * (dt ^ (row & 7)) + 8 * p;
  }

  State st;
#pragma unroll
  for (int dt = 0; dt < 8; ++dt) st.acc[dt] = f32x4{0.f, 0.f, 0.f, 0.f};
  st.m = -INFINITY; st.l = 0.f;

  wait_vm<DEPTH * 16>();
  __builtin_amdgcn_s_barrier();
#pragma unroll
  for (int kk = 0; kk < 4; ++kk) st.qf[kk] = lds_b128(smem + LDS_Q + ka[kk]);

  if (w == NW - 1) {
    f32x4 s = {0.f, 0.f, 0.f, 0.f};
#pragma unroll
    for (int kk = 0; kk < 4; ++kk) s = __builtin_amdgcn_mfma_f32_16x16x32_bf16(lds_b128(smem + LDS_KT + ka[kk]), st.qf[kk], s, 0, 0, 0);
    const u32x2 mk = *(const u32x2 LAS*)(smem + LDS_MASK + c * 32 + g * 8);
    const float mv[4] = {__uint_as_float(mk[0] << 16), __uint_as_float(mk[0] & 0xffff0000u), __uint_as_float(mk[1] << 16), __uint_as_float(mk[1] & 0xffff0000u)};
    float t[4];
#pragma unroll
    for (int r = 0; r < 4; ++r) t[r] = (s[r] * kScale + (1.0f - mv[r]) * -1000000000.0f) * kL2E;
    const float mx = xg_max(fmaxf(fmaxf(t[0], t[1]), fmaxf(t[2], t[3])));
    st.m = mx;
    float p[4];
#pragma unroll
    for (int r = 0; r < 4; ++r) { p[r] = __builtin_amdgcn_exp2f(t[r] - mx); st.l += p[r]; }
    const u32x2 pw = {cvtpk(p[0], p[1]), cvtpk(p[2], p[3])};
    const s16x4 pf = __builtin_bit_cast(s16x4, pw);
#pragma unroll
    for (int dt = 0; dt < 8; ++dt) st.acc[dt] = __builtin_amdgcn_mfma_f32_16x16x16bf16_1k(lds_tr(smem + LDS_VT + va[dt]), pf, st.acc[dt], 0, 0, 0);
  }

  const char LAS* kring = smem + w * RING;
  const char LAS* vring = kring + DEPTH * 8192;

  auto step = [&](int s, auto slot_c, auto wk_c, auto wv_c, auto issue_c) {
    constexpr int slot = decltype(slot_c)::value, WK = decltype(wk_c)::value, WV = decltype(wv_c)::value;
    constexpr bool ISSUE = decltype(issue_c)::value;
    SB();
    wait_vm<WK>();
    bf16x8 kf[2][4];
#pragma unroll
    for (int t = 0; t < 2; ++t)
#pragma unroll
      for (int kk = 0; kk < 4; ++kk) kf[t][kk] = lds_b128(kring + slot * 8192 + t * 4096 + ka[kk]);
    if constexpr (ISSUE) { wait_lgkm0(); issueK(s + DEPTH, slot); }
    SB();
    f32x4 sc[2];
#pragma unroll
    for (int t = 0; t < 2; ++t) {
      sc[t] = f32x4{0.f, 0.f, 0.f, 0.f};
#pragma unroll
      for (int kk = 0; kk < 4; ++kk) sc[t] = __builtin_amdgcn_mfma_f32_16x16x32_bf16(kf[t][kk], st.qf[kk], sc[t], 0, 0, 0);
    }
    float mx = fmaxf(fmaxf(fmaxf(sc[0][0], sc[0][1]), fmaxf(sc[0][2], sc[0][3])), fmaxf(fmaxf(sc[1][0], sc[1][1]), fmaxf(sc[1][2], sc[1][3])));
    mx = xg_max(mx) * kC;
    const float mn = fmaxf(st.m, mx);
    const float alpha = __builtin_amdgcn_exp2f(st.m - mn);
    st.m = mn;
    float p[8];
    float ps = 0.f;
#pragma unroll
    for (int t = 0; t < 2; ++t)
#pragma unroll
      for (int r = 0; r < 4; ++r) { p[4 * t + r] = __builtin_amdgcn_exp2f(fmaf(sc[t][r], kC, -mn)); ps += p[4 * t + r]; }
    st.l = fmaf(st.l, alpha, ps);
    const u32x4 pw = {cvtpk(p[0], p[1]), cvtpk(p[2], p[3]), cvtpk(p[4], p[5]), cvtpk(p[6], p[7])};
    const bf16x8 pf = __builtin_bit_cast(bf16x8, pw);
#pragma unroll
    for (int dt = 0; dt < 8; ++dt) st.acc[dt] *= alpha;
    SB();
    wait_vm<WV>();
    s16x4 vf[8][2];
#pragma unroll
    for (int dt = 0; dt < 8; ++dt)
#pragma unroll
      for (int t = 0; t < 2; ++t) vf[dt][t] = lds_tr(vring + slot * 8192 + t * 4096 + va[dt]);
    if constexpr (ISSUE) { wait_lgkm0(); issueV(s + DEPTH, slot); }
    SB();
#pragma unroll
    for (int dt = 0; dt < 8; ++dt) {
      const s16x8 a = {vf[dt][0][0], vf[dt][0][1], vf[dt][0][2], vf[dt][0][3], vf[dt][1][0], vf[dt][1][1], vf[dt][1][2], vf[dt][1][3]};
      st.acc[dt] = __builtin_amdgcn_mfma_f32_16x16x32_bf16(__builtin_bit_cast(bf16x8, a), pf, st.acc[dt], 0, 0, 0);
    }
  };
  using std::integral_constant;
#define IC(x) integral_constant<int, (x)>{}
#define BC(x) integral_constant<bool, (x)>{}
  constexpr int WS = 8 * (2 * DEPTH - 1);
  if constexpr (DEPTH == 1) {
    for (int s = 0; s < NS - 1; ++s) step(s, IC(0), IC(WS), IC(WS), BC(true));
    step(NS - 1, IC(0), IC(8), IC(0), BC(false));
  } else if constexpr (DEPTH == 2) {
    for (int s = 0; s < NS - 2; s += 2) { step(s, IC(0), IC(WS), IC(WS), BC(true)); step(s + 1, IC(1), IC(WS), IC(WS), BC(true)); }
    step(NS - 2, IC(0), IC(24), IC(16), BC(false));
    step(NS - 1, IC(1), IC(8), IC(0), BC(false));
  } else if constexpr (DEPTH == 4) {
    for (int s = 0; s < NS - 4; s += 4) {
      step(s, IC(0), IC(WS), IC(WS), BC(true)); step(s + 1, IC(1), IC(WS), IC(WS), BC(true));
      step(s + 2, IC(2), IC(WS), IC(WS), BC(true)); step(s + 3, IC(3), IC(WS), IC(WS), BC(true));
    }
    step(NS - 4, IC(0), IC(56), IC(48), BC(false));
    step(NS - 3, IC(1), IC(40), IC(32), BC(false));
    step(NS - 2, IC(2), IC(24), IC(16), BC(false));
    step(NS - 1, IC(3), IC(8), IC(0), BC(false));
  }

  st.l = xg_sum(st.l);
  {
    char LAS* mr = smem + w * RING;
#pragma unroll
    for (int dt = 0; dt < 8; ++dt) *(f32x4 LAS*)(mr + c * MROW + (16 * dt + 4 * g) * 4) = st.acc[dt];
    if (g == 0) { *(float LAS*)(mr + 16 * MROW + c * 8) = st.m; *(float LAS*)(mr + 16 * MROW + c * 8 + 4) = st.l; }
  }
  __syncthreads();
  if (tid < 256) {
    const int qi = tid >> 4, dc = tid & 15;
    float mw[NW], lw[NW], M = -INFINITY;
#pragma unroll
    for (int x = 0; x < NW; ++x) {
      mw[x] = *(const float LAS*)(smem + x * RING + 16 * MROW + qi * 8);
      lw[x] = *(const float LAS*)(smem + x * RING + 16 * MROW + qi * 8 + 4);
      M = fmaxf(M, mw[x]);
    }
    float L = 0.f;
    f32x4 o0 = {0.f, 0.f, 0.f, 0.f}, o1 = {0.f, 0.f, 0.f, 0.f};
#pragma unroll
    for (int x = 0; x < NW; ++x) {
      const float f = __builtin_amdgcn_exp2f(mw[x] - M);
      L = fmaf(f, lw[x], L);
      const f32x4 a0 = *(const f32x4 LAS*)(smem + x * RING + qi * MROW + dc * 32);
      const f32x4 a1 = *(const f32x4 LAS*)(smem + x * RING + qi * MROW + dc * 32 + 16);
      o0 += f * a0; o1 += f * a1;
    }
    const float inv = 1.0f / L;
    const u32x4 ow = {cvtpk(o0[0] * inv, o0[1] * inv), cvtpk(o0[2] * inv, o0[3] * inv), cvtpk(o1[0] * inv, o1[1] * inv), cvtpk(o1[2] * inv, o1[3] * inv)};
    *(u32x4*)(out + ((size_t)bh * T_ + qi) * D_ + dc * 8) = ow;
  }
}

extern "C" void kernel_launch(void* const* d_in, const int* in_sizes, int n_in, void* d_out, int out_size, void* d_ws, size_t ws_size,
                              hipStream_t stream) {
  (void)in_sizes; (void)n_in; (void)out_size; (void)d_ws; (void)ws_size;
  (void)hipFuncSetAttribute((const void*)tree_attn_kernel, hipFuncAttributeMaxDynamicSharedMemorySize, LDS_TOTAL);
  hipLaunchKernelGGL(tree_attn_kernel, dim3(B_ * H_), dim3(NW * 64), LDS_TOTAL, stream,
                     (const uint16_t*)d_in[0], (const uint16_t*)d_in[1], (const uint16_t*)d_in[2], (const uint16_t*)d_in[3],
                     (const uint16_t*)d_in[4], (const uint16_t*)d_in[5], (uint16_t*)d_out);
}
```

```cpp
#include <hip/hip_runtime.h>
#include <hip/hip_bf16.h>
#include <stdint.h>
#include <utility>

namespace ta {

constexpr int B_ = 8, H_ = 32, T_ = 16, C_ = 4096, D_ = 128;
constexpr int MROW = 528;

typedef __attribute__((ext_vector_type(8))) __bf16 bf16x8;
typedef __attribute__((ext_vector_type(4))) short s16x4;
typedef __attribute__((ext_vector_type(8))) short s16x8;
typedef __attribute__((ext_vector_type(4))) float f32x4;
typedef __attribute__((ext_vector_type(4))) unsigned u32x4;
typedef __attribute__((ext_vector_type(2))) unsigned u32x2;
#define LAS __attribute__((address_space(3)))
#define SB() __builtin_amdgcn_sched_barrier(0)

__device__ __forceinline__ unsigned cvtpk(float lo, float hi) {
  typedef __attribute__((ext_vector_type(2))) __bf16 bf16x2;
  typedef __attribute__((ext_vector_type(2))) float f32x2;
  f32x2 v = {lo, hi};
  bf16x2 r = __builtin_convertvector(v, bf16x2);
  return __builtin_bit_cast(unsigned, r);
}
__device__ __forceinline__ float xg_max(float x) {
  auto s = __builtin_amdgcn_permlane16_swap(__float_as_uint(x), __float_as_uint(x), false, false);
  x = fmaxf(__uint_as_float(s[0]), __uint_as_float(s[1]));
  auto t = __builtin_amdgcn_permlane32_swap(__float_as_uint(x), __float_as_uint(x), false, false);
  return fmaxf(__uint_as_float(t[0]), __uint_as_float(t[1]));
}
__device__ __forceinline__ float xg_sum(float x) {
  auto s = __builtin_amdgcn_permlane16_swap(__float_as_uint(x), __float_as_uint(x), false, false);
  x = __uint_as_float(s[0]) + __uint_as_float(s[1]);
  auto t = __builtin_amdgcn_permlane32_swap(__float_as_uint(x), __float_as_uint(x), false, false);
  return __uint_as_float(t[0]) + __uint_as_float(t[1]);
}

template <int NT>
__device__ __forceinline__ void dma_tile(unsigned m0v, unsigned v0, unsigned v1, unsigned v2, unsigned v3,
                                         __amdgpu_buffer_rsrc_t srd, unsigned soff) {
  if constexpr (NT)
    asm volatile(
      "s_mov_b32 m0, %0\n\t"
      "s_nop 0\n\t"
      "buffer_load_dwordx4 %1, %5, %6 offen nt lds\n\t"
      "buffer_load_dwordx4 %2, %5, %6 offen offset:1024 nt lds\n\t"
      "buffer_load_dwordx4 %3, %5, %6 offen offset:2048 nt lds\n\t"
      "buffer_load_dwordx4 %4, %5, %6 offen offset:3072 nt lds"
      :: "s"(m0v), "v"(v0), "v"(v1), "v"(v2), "v"(v3), "s"(srd), "s"(soff) : "memory");
  else
    asm volatile(
      "s_mov_b32 m0, %0\n\t"
      "s_nop 0\n\t"
      "buffer_load_dwordx4 %1, %5, %6 offen lds\n\t"
      "buffer_load_dwordx4 %2, %5, %6 offen offset:1024 lds\n\t"
      "buffer_load_dwordx4 %3, %5, %6 offen offset:2048 lds\n\t"
      "buffer_load_dwordx4 %4, %5, %6 offen offset:3072 lds"
      :: "s"(m0v), "v"(v0), "v"(v1), "v"(v2), "v"(v3), "s"(srd), "s"(soff) : "memory");
}
template <int N> __device__ __forceinline__ void wait_vm() { static_assert(N >= 0 && N <= 63, "vmcnt"); asm volatile("s_waitcnt vmcnt(%0)" :: "i"(N) : "memory"); }
__device__ __forceinline__ void wait_lgkm0() { asm volatile("s_waitcnt lgkmcnt(0)" ::: "memory"); }

constexpr float kScale = 0.088388347648318440f;
constexpr float kL2E = 1.4426950408889634f;
constexpr float kC = kScale * kL2E;

__device__ __forceinline__ bf16x8 lds_b128(const char LAS* p) { return *(const bf16x8 LAS*)p; }
__device__ __forceinline__ s16x4 lds_tr(const char LAS* p) { return __builtin_amdgcn_ds_read_tr16_b64_v4i16((s16x4 LAS*)p); }

template <class F, int... I> __device__ __forceinline__ void sfor_impl(F&& f, std::integer_sequence<int, I...>) { (f(std::integral_constant<int, I>{}), ...); }
template <int N, class F> __device__ __forceinline__ void sfor(F&& f) { sfor_impl(f, std::make_integer_sequence<int, N>{}); }

template <int NW_, int TK_, int DEPTH_, int ORD_ = 0, int NT_ = 1, int STAMP_ = 0, int CRS_ = 0>
struct Cfg {
  static constexpr int NW = NW_, TK = TK_, DEPTH = DEPTH_, ORD = ORD_, NT = NT_, STAMP = STAMP_, CRS = CRS_;
  static constexpr int SLOT = TK * 4096;
  static constexpr int RING = DEPTH * 2 * SLOT;
  static constexpr int NS = C_ / (16 * TK) / NW;
  static constexpr int MRG = (16 * MROW + 128 + 255) / 256 * 256;
  static constexpr int WREG = RING > MRG ? RING : MRG;
  static constexpr int LDS_Q = NW * WREG, LDS_KT = LDS_Q + 4096, LDS_VT = LDS_KT + 4096, LDS_MASK = LDS_VT + 4096;
  static constexpr int LDS_USED = LDS_MASK + 1024;
  static constexpr int LDS_TOTAL = LDS_USED > 83968 ? LDS_USED : 83968;
  static_assert(LDS_TOTAL <= 163840, "LDS");
  static_assert(DEPTH * 2 * TK * 4 <= 63 - 16 / NW + 0 || DEPTH * 2 * TK * 4 <= 63, "vmcnt range");
};

template <class C>
__global__ __launch_bounds__(C::NW * 64, C::NW >= 16 ? 4 : 2) void tree_attn(
    const uint16_t* __restrict__ q, const uint16_t* __restrict__ kc, const uint16_t* __restrict__ vc,
    const uint16_t* __restrict__ kt, const uint16_t* __restrict__ vt, const uint16_t* __restrict__ mask,
    uint16_t* __restrict__ out, unsigned long long* __restrict__ stamps) {
  constexpr int NW = C::NW, TK = C::TK, DEPTH = C::DEPTH, NS = C::NS, SLOT = C::SLOT, WREG = C::WREG, G = 4 * TK;
  extern __shared__ __attribute__((aligned(16))) char smem_[];
  char LAS* smem = (char LAS*)smem_;
  const int tid = threadIdx.x, lane = tid & 63;
  const int w = __builtin_amdgcn_readfirstlane(tid >> 6);
  const int bh = blockIdx.x, b = bh >> 5;
  const unsigned g = lane >> 4, c = lane & 15;
  const unsigned lds0 = (unsigned)(size_t)smem;
  unsigned long long t0 = 0; unsigned pv = 0;
  if constexpr (C::STAMP) t0 = __builtin_amdgcn_s_memrealtime();

  const __amdgpu_buffer_rsrc_t srdK = __builtin_amdgcn_make_buffer_rsrc((void*)(kc + (size_t)bh * C_ * D_), 0, C_ * D_ * 2, 0x00020000);
  const __amdgpu_buffer_rsrc_t srdV = __builtin_amdgcn_make_buffer_rsrc((void*)(vc + (size_t)bh * C_ * D_), 0, C_ * D_ * 2, 0x00020000);

  unsigned vk[4], vv[2];
#pragma unroll
  for (int i = 0; i < 4; ++i) vk[i] = g * 256 + 16 * ((c ^ g) ^ (4 * i));
#pragma unroll
  for (int j = 0; j < 2; ++j) vv[j] = g * 256 + 16 * (c ^ (2 * g) ^ (8 * j));

  const unsigned ring = lds0 + w * WREG;
  auto step_off = [&](int s) -> unsigned { return C::ORD == 0 ? (unsigned)(s * NW + w) * SLOT : (unsigned)(w * NS + s) * SLOT; };
  auto issueK = [&](int s, int slot) {
    const unsigned o = step_off(s);
#pragma unroll
    for (int t = 0; t < TK; ++t) dma_tile<C::NT>(ring + slot * SLOT + t * 4096, vk[0], vk[1], vk[2], vk[3], srdK, o + t * 4096);
  };
  auto issueV = [&](int s, int slot) {
    const unsigned o = step_off(s);
#pragma unroll
    for (int t = 0; t < TK; ++t) dma_tile<C::NT>(ring + DEPTH * SLOT + slot * SLOT + t * 4096, vv[0], vv[1], vv[0], vv[1], srdV, o + t * 4096);
  };

  {
    const size_t tb = (size_t)bh * T_ * D_ * 2;
    constexpr int PPW = NW <= 16 ? 16 / NW : 1;
#pragma unroll
    for (int j = 0; j < PPW; ++j) {
      const int pi = (w * PPW + j) & 15;
      const int kind = pi & 3, rg = pi >> 2;
      const unsigned row = 4 * rg + g;
      const char* src; unsigned dst;
      if (kind == 0)      { src = (const char*)q + tb + row * 256 + 16 * (c ^ row);              dst = C::LDS_Q + rg * 1024; }
      else if (kind == 1) { src = (const char*)kt + tb + row * 256 + 16 * (c ^ row);             dst = C::LDS_KT + rg * 1024; }
      else if (kind == 2) { src = (const char*)vt + tb + row * 256 + 16 * (c ^ (2 * (row & 7))); dst = C::LDS_VT + rg * 1024; }
      else                { src = (const char*)mask + (size_t)b * 512 + (lane & 31) * 16;        dst = C::LDS_MASK; }
      const unsigned m0v = __builtin_amdgcn_readfirstlane(lds0 + dst);
      asm volatile("s_nop 4\n\ts_mov_b32 m0, %0\n\ts_nop 0\n\tglobal_load_lds_dwordx4 %1, off" :: "s"(m0v), "v"(src) : "memory");
    }
  }
#pragma unroll
  for (int s = 0; s < DEPTH; ++s) { issueK(s, s); issueV(s, s); }

  unsigned ka[4], va[8];
#pragma unroll
  for (int kk = 0; kk < 4; ++kk) ka[kk] = c * 256 + 16 * ((g ^ c) ^ (4 * kk));
  {
    const unsigned qq = c >> 2, p = c & 3, row = 4 * g + qq;
#pragma unroll
    for (int dt = 0; dt < 8; ++dt) va[dt] = row * 256 + 32 * (dt ^ (row & 7)) + 8 * p;
  }

  bf16x8 qf[4];
  f32x4 acc[8];
#pragma unroll
  for (int dt = 0; dt < 8; ++dt) acc[dt] = f32x4{0.f, 0.f, 0.f, 0.f};
  float m = -INFINITY, l = 0.f;

  wait_vm<DEPTH * 2 * G>();
  if constexpr (C::STAMP) { unsigned t = (unsigned)__builtin_amdgcn_s_memrealtime(); asm volatile("s_waitcnt lgkmcnt(0)" ::: "memory"); pv = (lane == 17) ? t : pv; pv = (lane == 18) ? (unsigned)t0 : pv; }
  __builtin_amdgcn_s_barrier();
#pragma unroll
  for (int kk = 0; kk < 4; ++kk) qf[kk] = lds_b128(smem + C::LDS_Q + ka[kk]);

  if (w == NW - 1) {
    f32x4 s = {0.f, 0.f, 0.f, 0.f};
#pragma unroll
    for (int kk = 0; kk < 4; ++kk) s = __builtin_amdgcn_mfma_f32_16x16x32_bf16(lds_b128(smem + C::LDS_KT + ka[kk]), qf[kk], s, 0, 0, 0);
    const u32x2 mk = *(const u32x2 LAS*)(smem + C::LDS_MASK + c * 32 + g * 8);
    const float mv[4] = {__uint_as_float(mk[0] << 16), __uint_as_float(mk[0] & 0xffff0000u), __uint_as_float(mk[1] << 16), __uint_as_float(mk[1] & 0xffff0000u)};
    float t[4];
#pragma unroll
    for (int r = 0; r < 4; ++r) t[r] = (s[r] * kScale + (1.0f - mv[r]) * -1000000000.0f) * kL2E;
    const float mx = xg_max(fmaxf(fmaxf(t[0], t[1]), fmaxf(t[2], t[3])));
    m = mx;
    float p[4];
#pragma unroll
    for (int r = 0; r < 4; ++r) { p[r] = __builtin_amdgcn_exp2f(t[r] - mx); l += p[r]; }
    const u32x2 pw = {cvtpk(p[0], p[1]), cvtpk(p[2], p[3])};
    const s16x4 pf = __builtin_bit_cast(s16x4, pw);
#pragma unroll
    for (int dt = 0; dt < 8; ++dt) acc[dt] = __builtin_amdgcn_mfma_f32_16x16x16bf16_1k(lds_tr(smem + C::LDS_VT + va[dt]), pf, acc[dt], 0, 0, 0);
  }

  const char LAS* kring = smem + w * WREG;
  const char LAS* vring = kring + DEPTH * SLOT;

  auto step = [&](int s, auto slot_c, auto wk_c, auto wv_c, auto issue_c) {
    constexpr int slot = decltype(slot_c)::value, WK = decltype(wk_c)::value, WV = decltype(wv_c)::value;
    constexpr bool ISSUE = decltype(issue_c)::value;
    SB();
    wait_vm<WK>();
    if constexpr (C::STAMP) { if ((s % (NS / 16 > 0 ? NS / 16 : 1)) == 0) { unsigned t = (unsigned)__builtin_amdgcn_s_memrealtime(); asm volatile("s_waitcnt lgkmcnt(0)" ::: "memory"); pv = (lane == s / (NS / 16 > 0 ? NS / 16 : 1)) ? t : pv; } }
    bf16x8 kf[TK][4];
#pragma unroll
    for (int t = 0; t < TK; ++t)
#pragma unroll
      for (int kk = 0; kk < 4; ++kk) kf[t][kk] = lds_b128(kring + slot * SLOT + t * 4096 + ka[kk]);
    if constexpr (ISSUE) { wait_lgkm0(); issueK(s + DEPTH, slot); }
    SB();
    f32x4 sc[TK];
#pragma unroll
    for (int t = 0; t < TK; ++t) {
      sc[t] = f32x4{0.f, 0.f, 0.f, 0.f};
#pragma unroll
      for (int kk = 0; kk < 4; ++kk) sc[t] = __builtin_amdgcn_mfma_f32_16x16x32_bf16(kf[t][kk], qf[kk], sc[t], 0, 0, 0);
    }
    float mx = fmaxf(fmaxf(sc[0][0], sc[0][1]), fmaxf(sc[0][2], sc[0][3]));
    if constexpr (TK == 2) mx = fmaxf(mx, fmaxf(fmaxf(sc[1][0], sc[1][1]), fmaxf(sc[1][2], sc[1][3])));
    mx = xg_max(mx) * kC;
    float mn;
    if constexpr (C::CRS) {
      if (__builtin_amdgcn_ballot_w64(mx > m) != 0) {
        mn = fmaxf(m, mx);
        const float alpha = __builtin_amdgcn_exp2f(m - mn);
        m = mn;
        l *= alpha;
#pragma unroll
        for (int dt = 0; dt < 8; ++dt) acc[dt] *= alpha;
      }
      mn = m;
    }
    float alpha = 1.f;
    if constexpr (!C::CRS) {
      mn = fmaxf(m, mx);
      alpha = __builtin_amdgcn_exp2f(m - mn);
      m = mn;
    }
    float p[4 * TK];
    float ps = 0.f;
#pragma unroll
    for (int t = 0; t < TK; ++t)
#pragma unroll
      for (int r = 0; r < 4; ++r) { p[4 * t + r] = __builtin_amdgcn_exp2f(fmaf(sc[t][r], kC, -mn)); ps += p[4 * t + r]; }
    if constexpr (C::CRS) l += ps; else l = fmaf(l, alpha, ps);
    if constexpr (!C::CRS) {
#pragma unroll
      for (int dt = 0; dt < 8; ++dt) acc[dt] *= alpha;
    }
    unsigned pw[2 * TK];
#pragma unroll
    for (int i = 0; i < 2 * TK; ++i) pw[i] = cvtpk(p[2 * i], p[2 * i + 1]);
    SB();
    wait_vm<WV>();
    s16x4 vf[8][TK];
#pragma unroll
    for (int dt = 0; dt < 8; ++dt)
#pragma unroll
      for (int t = 0; t < TK; ++t) vf[dt][t] = lds_tr(vring + slot * SLOT + t * 4096 + va[dt]);
    if constexpr (ISSUE) { wait_lgkm0(); issueV(s + DEPTH, slot); }
    SB();
    if constexpr (TK == 2) {
      const u32x4 pq = {pw[0], pw[1], pw[2], pw[3]};
      const bf16x8 pf = __builtin_bit_cast(bf16x8, pq);
#pragma unroll
      for (int dt = 0; dt < 8; ++dt) {
        const s16x8 a = {vf[dt][0][0], vf[dt][0][1], vf[dt][0][2], vf[dt][0][3], vf[dt][1][0], vf[dt][1][1], vf[dt][1][2], vf[dt][1][3]};
        acc[dt] = __builtin_amdgcn_mfma_f32_16x16x32_bf16(__builtin_bit_cast(bf16x8, a), pf, acc[dt], 0, 0, 0);
      }
    } else {
      const u32x2 pq = {pw[0], pw[1]};
      const s16x4 pf = __builtin_bit_cast(s16x4, pq);
#pragma unroll
      for (int dt = 0; dt < 8; ++dt) acc[dt] = __builtin_amdgcn_mfma_f32_16x16x16bf16_1k(vf[dt][0], pf, acc[dt], 0, 0, 0);
    }
  };
  using std::integral_constant;
  constexpr int WS = G * (2 * DEPTH - 1);
  constexpr int R = (NS - DEPTH) / DEPTH;
  for (int r = 0; r < R; ++r)
    sfor<DEPTH>([&](auto i) { step(r * DEPTH + i.value, i, integral_constant<int, WS>{}, integral_constant<int, WS>{}, integral_constant<bool, true>{}); });
  sfor<NS - R * DEPTH>([&](auto i) {
    constexpr int s = R * DEPTH + i.value;
    constexpr int last = (s + DEPTH - 1 < NS - 1) ? s + DEPTH - 1 : NS - 1;
    constexpr bool iss = s + DEPTH < NS;
    step(s, integral_constant<int, s % DEPTH>{}, integral_constant<int, G * (1 + 2 * (last - s))>{},
         integral_constant<int, G * (2 * (last - s) + (iss ? 1 : 0))>{}, integral_constant<bool, iss>{});
  });

  if constexpr (C::STAMP) { unsigned t = (unsigned)__builtin_amdgcn_s_memrealtime(); asm volatile("s_waitcnt lgkmcnt(0)" ::: "memory"); pv = (lane == 16) ? t : pv; }
  l = xg_sum(l);
  {
    char LAS* mr = smem + w * WREG;
#pragma unroll
    for (int dt = 0; dt < 8; ++dt) *(f32x4 LAS*)(mr + c * MROW + (16 * dt + 4 * g) * 4) = acc[dt];
    if (g == 0) { *(float LAS*)(mr + 16 * MROW + c * 8) = m; *(float LAS*)(mr + 16 * MROW + c * 8 + 4) = l; }
  }
  __syncthreads();
  if (tid < 256) {
    const int qi = tid >> 4, dc = tid & 15;
    float mw[NW], lw[NW], M = -INFINITY;
#pragma unroll
    for (int x = 0; x < NW; ++x) {
      mw[x] = *(const float LAS*)(smem + x * WREG + 16 * MROW + qi * 8);
      lw[x] = *(const float LAS*)(smem + x * WREG + 16 * MROW + qi * 8 + 4);
      M = fmaxf(M, mw[x]);
    }
    float L = 0.f;
    f32x4 o0 = {0.f, 0.f, 0.f, 0.f}, o1 = {0.f, 0.f, 0.f, 0.f};
#pragma unroll
    for (int x = 0; x < NW; ++x) {
      const float f = __builtin_amdgcn_exp2f(mw[x] - M);
      L = fmaf(f, lw[x], L);
      const f32x4 a0 = *(const f32x4 LAS*)(smem + x * WREG + qi * MROW + dc * 32);
      const f32x4 a1 = *(const f32x4 LAS*)(smem + x * WREG + qi * MROW + dc * 32 + 16);
      o0 += f * a0; o1 += f * a1;
    }
    const float inv = 1.0f / L;
    const u32x4 ow = {cvtpk(o0[0] * inv, o0[1] * inv), cvtpk(o0[2] * inv, o0[3] * inv), cvtpk(o1[0] * inv, o1[1] * inv), cvtpk(o1[2] * inv, o1[3] * inv)};
    *(u32x4*)(out + ((size_t)bh * T_ + qi) * D_ + dc * 8) = ow;
  }
  if constexpr (C::STAMP) {
    unsigned long long t1 = __builtin_amdgcn_s_memrealtime();
    unsigned xcc; asm volatile("s_getreg_b32 %0, hwreg(HW_REG_XCC_ID)" : "=s"(xcc));
    if (tid == 0) { stamps[2 * bh] = t0; stamps[2 * bh + 1] = t1; stamps[8192 + bh] = xcc & 0xf; }
    if (w == 0 && lane < 32) stamps[16384 + bh * 32 + lane] = pv;
  }
}

}

extern "C" void kernel_launch(void* const* d_in, const int* in_sizes, int n_in, void* d_out, int out_size, void* d_ws, size_t ws_size,
                              hipStream_t stream) {
  (void)in_sizes; (void)n_in; (void)out_size; (void)ws_size;
  using C = ta::Cfg<4, 1, 2, 0, 1, 0, 0>;
  (void)hipFuncSetAttribute((const void*)ta::tree_attn<C>, hipFuncAttributeMaxDynamicSharedMemorySize, C::LDS_TOTAL);
  hipLaunchKernelGGL((ta::tree_attn<C>), dim3(256), dim3(C::NW * 64), C::LDS_TOTAL, stream,
                     (const uint16_t*)d_in[0], (const uint16_t*)d_in[1], (const uint16_t*)d_in[2], (const uint16_t*)d_in[3],
                     (const uint16_t*)d_in[4], (const uint16_t*)d_in[5], (uint16_t*)d_out, (unsigned long long*)d_ws);
}
```

```cpp
#include <hip/hip_runtime.h>
#include <hip/hip_bf16.h>
#include <stdint.h>
#include <utility>

namespace ta {

constexpr int B_ = 8, H_ = 32, T_ = 16, C_ = 4096, D_ = 128;
constexpr int MROW = 528;

typedef __attribute__((ext_vector_type(8))) __bf16 bf16x8;
typedef __attribute__((ext_vector_type(4))) short s16x4;
typedef __attribute__((ext_vector_type(8))) short s16x8;
typedef __attribute__((ext_vector_type(4))) float f32x4;
typedef __attribute__((ext_vector_type(4))) unsigned u32x4;
typedef __attribute__((ext_vector_type(2))) unsigned u32x2;
#define LAS __attribute__((address_space(3)))
#define SB() __builtin_amdgcn_sched_barrier(0)

__device__ __forceinline__ unsigned cvtpk(float lo, float hi) {
  typedef __attribute__((ext_vector_type(2))) __bf16 bf16x2;
  typedef __attribute__((ext_vector_type(2))) float f32x2;
  f32x2 v = {lo, hi};
  bf16x2 r = __builtin_convertvector(v, bf16x2);
  return __builtin_bit_cast(unsigned, r);
}
__device__ __forceinline__ float xg_max(float x) {
  auto s = __builtin_amdgcn_permlane16_swap(__float_as_uint(x), __float_as_uint(x), false, false);
  x = fmaxf(__uint_as_float(s[0]), __uint_as_float(s[1]));
  auto t = __builtin_amdgcn_permlane32_swap(__float_as_uint(x), __float_as_uint(x), false, false);
  return fmaxf(__uint_as_float(t[0]), __uint_as_float(t[1]));
}
__device__ __forceinline__ float xg_sum(float x) {
  auto s = __builtin_amdgcn_permlane16_swap(__float_as_uint(x), __float_as_uint(x), false, false);
  x = __uint_as_float(s[0]) + __uint_as_float(s[1]);
  auto t = __builtin_amdgcn_permlane32_swap(__float_as_uint(x), __float_as_uint(x), false, false);
  return __uint_as_float(t[0]) + __uint_as_float(t[1]);
}

template <int NT>
__device__ __forceinline__ void dma_tile(unsigned m0v, unsigned v0, unsigned v1, unsigned v2, unsigned v3,
                                         __amdgpu_buffer_rsrc_t srd, unsigned soff) {
  if constexpr (NT)
    asm volatile(
      "s_mov_b32 m0, %0\n\t"
      "s_nop 0\n\t"
      "buffer_load_dwordx4 %1, %5, %6 offen nt lds\n\t"
      "buffer_load_dwordx4 %2, %5, %6 offen offset:1024 nt lds\n\t"
      "buffer_load_dwordx4 %3, %5, %6 offen offset:2048 nt lds\n\t"
      "buffer_load_dwordx4 %4, %5, %6 offen offset:3072 nt lds"
      :: "s"(m0v), "v"(v0), "v"(v1), "v"(v2), "v"(v3), "s"(srd), "s"(soff) : "memory");
  else
    asm volatile(
      "s_mov_b32 m0, %0\n\t"
      "s_nop 0\n\t"
      "buffer_load_dwordx4 %1, %5, %6 offen lds\n\t"
      "buffer_load_dwordx4 %2, %5, %6 offen offset:1024 lds\n\t"
      "buffer_load_dwordx4 %3, %5, %6 offen offset:2048 lds\n\t"
      "buffer_load_dwordx4 %4, %5, %6 offen offset:3072 lds"
      :: "s"(m0v), "v"(v0), "v"(v1), "v"(v2), "v"(v3), "s"(srd), "s"(soff) : "memory");
}
template <int N> __device__ __forceinline__ void wait_vm() { static_assert(N >= 0 && N <= 63, "vmcnt"); asm volatile("s_waitcnt vmcnt(%0)" :: "i"(N) : "memory"); }
__device__ __forceinline__ void wait_lgkm0() { asm volatile("s_waitcnt lgkmcnt(0)" ::: "memory"); }

constexpr float kScale = 0.088388347648318440f;
constexpr float kL2E = 1.4426950408889634f;
constexpr float kC = kScale * kL2E;

__device__ __forceinline__ bf16x8 lds_b128(const char LAS* p) { return *(const bf16x8 LAS*)p; }
__device__ __forceinline__ s16x4 lds_tr(const char LAS* p) { return __builtin_amdgcn_ds_read_tr16_b64_v4i16((s16x4 LAS*)p); }

template <class F, int... I> __device__ __forceinline__ void sfor_impl(F&& f, std::integer_sequence<int, I...>) { (f(std::integral_constant<int, I>{}), ...); }
template <int N, class F> __device__ __forceinline__ void sfor(F&& f) { sfor_impl(f, std::make_integer_sequence<int, N>{}); }

template <int NW_, int TK_, int DEPTH_, int ORD_ = 0, int NT_ = 1, int STAMP_ = 0, int CRS_ = 0, int PACE_ = 0, int PRO_ = 0, int TOUCH_ = 0, int OST_ = 0>
struct Cfg {
  static constexpr int NW = NW_, TK = TK_, DEPTH = DEPTH_, ORD = ORD_, NT = NT_, STAMP = STAMP_, CRS = CRS_, PACE = PACE_, PRO = PRO_, TOUCH = TOUCH_, OST = OST_;
  static constexpr int SLOT = TK * 4096;
  static constexpr int RING = DEPTH * 2 * SLOT;
  static constexpr int NS = C_ / (16 * TK) / NW;
  static constexpr int MRG = (16 * MROW + 128 + 255) / 256 * 256;
  static constexpr int WREG = RING > MRG ? RING : MRG;
  static constexpr int LDS_Q = NW * WREG, LDS_KT = LDS_Q + 4096, LDS_VT = LDS_KT + 4096, LDS_MASK = LDS_VT + 4096;
  static constexpr int LDS_DUMMY = LDS_MASK + 1024;
  static constexpr int LDS_USED = LDS_DUMMY + 1024;
  static constexpr int LDS_TOTAL = LDS_USED > 83968 ? LDS_USED : 83968;
  static_assert(LDS_TOTAL <= 163840, "LDS");
  static_assert(DEPTH * 2 * TK * 4 <= 63 - 16 / NW + 0 || DEPTH * 2 * TK * 4 <= 63, "vmcnt range");
};

template <class C>
__global__ __launch_bounds__(C::NW * 64, C::NW >= 16 ? 4 : 2) void tree_attn(
    const uint16_t* __restrict__ q, const uint16_t* __restrict__ kc, const uint16_t* __restrict__ vc,
    const uint16_t* __restrict__ kt, const uint16_t* __restrict__ vt, const uint16_t* __restrict__ mask,
    uint16_t* __restrict__ out, unsigned long long* __restrict__ stamps) {
  constexpr int NW = C::NW, TK = C::TK, DEPTH = C::DEPTH, NS = C::NS, SLOT = C::SLOT, WREG = C::WREG, G = 4 * TK;
  extern __shared__ __attribute__((aligned(16))) char smem_[];
  char LAS* smem = (char LAS*)smem_;
  const int tid = threadIdx.x, lane = tid & 63;
  const int w = __builtin_amdgcn_readfirstlane(tid >> 6);
  const int bh = blockIdx.x, b = bh >> 5;
  const unsigned g = lane >> 4, c = lane & 15;
  const unsigned lds0 = (unsigned)(size_t)smem;
  unsigned long long t0 = 0; unsigned pv = 0;
  if constexpr (C::STAMP || C::PACE) t0 = __builtin_amdgcn_s_memrealtime();

  const __amdgpu_buffer_rsrc_t srdK = __builtin_amdgcn_make_buffer_rsrc((void*)(kc + (size_t)bh * C_ * D_), 0, C_ * D_ * 2, 0x00020000);
  const __amdgpu_buffer_rsrc_t srdV = __builtin_amdgcn_make_buffer_rsrc((void*)(vc + (size_t)bh * C_ * D_), 0, C_ * D_ * 2, 0x00020000);

  unsigned vk[4], vv[2];
#pragma unroll
  for (int i = 0; i < 4; ++i) vk[i] = g * 256 + 16 * ((c ^ g) ^ (4 * i));
#pragma unroll
  for (int j = 0; j < 2; ++j) vv[j] = g * 256 + 16 * (c ^ (2 * g) ^ (8 * j));

  const unsigned ring = lds0 + w * WREG;
  auto step_off = [&](int s) -> unsigned { return C::ORD == 0 ? (unsigned)(s * NW + w) * SLOT : (unsigned)(w * NS + s) * SLOT; };
  auto issueK = [&](int s, int slot) {
    const unsigned o = step_off(s);
#pragma unroll
    for (int t = 0; t < TK; ++t) dma_tile<(C::NT == 1 || C::NT == 2)>(ring + slot * SLOT + t * 4096, vk[0], vk[1], vk[2], vk[3], srdK, o + t * 4096);
  };
  auto issueV = [&](int s, int slot) {
    const unsigned o = step_off(s);
#pragma unroll
    for (int t = 0; t < TK; ++t) dma_tile<(C::NT == 1 || C::NT == 3)>(ring + DEPTH * SLOT + slot * SLOT + t * 4096, vv[0], vv[1], vv[0], vv[1], srdV, o + t * 4096);
  };

  constexpr int PPW = NW <= 16 ? 16 / NW : 1;
  auto pieces = [&]() {
    const size_t tb = (size_t)bh * T_ * D_ * 2;
#pragma unroll
    for (int j = 0; j < PPW; ++j) {
      const int pi = (w * PPW + j) & 15;
      const int kind = pi & 3, rg = pi >> 2;
      const unsigned row = 4 * rg + g;
      const char* src; unsigned dst;
      if (kind == 0)      { src = (const char*)q + tb + row * 256 + 16 * (c ^ row);              dst = C::LDS_Q + rg * 1024; }
      else if (kind == 1) { src = (const char*)kt + tb + row * 256 + 16 * (c ^ row);             dst = C::LDS_KT + rg * 1024; }
      else if (kind == 2) { src = (const char*)vt + tb + row * 256 + 16 * (c ^ (2 * (row & 7))); dst = C::LDS_VT + rg * 1024; }
      else                { src = (const char*)mask + (size_t)b * 512 + (lane & 31) * 16;        dst = C::LDS_MASK; }
      const unsigned m0v = __builtin_amdgcn_readfirstlane(lds0 + dst);
      asm volatile("s_nop 4\n\ts_mov_b32 m0, %0\n\ts_nop 0\n\tglobal_load_lds_dwordx4 %1, off" :: "s"(m0v), "v"(src) : "memory");
    }
    if constexpr (C::TOUCH) {
      const char* src = (const char*)out + tb + lane * 16;
      const unsigned m0v = lds0 + C::LDS_DUMMY;
      asm volatile("s_mov_b32 m0, %0\n\ts_nop 0\n\tglobal_load_lds_dwordx4 %1, off" :: "s"(m0v), "v"(src) : "memory");
    }
  };
  if constexpr (C::PRO == 0) {
    pieces();
#pragma unroll
    for (int s = 0; s < DEPTH; ++s) { issueK(s, s); issueV(s, s); }
  } else {
    issueK(0, 0); issueV(0, 0);
    pieces();
#pragma unroll
    for (int s = 1; s < DEPTH; ++s) { issueK(s, s); issueV(s, s); }
  }
  constexpr int PWAIT = C::PRO == 0 ? DEPTH * 2 * G : (DEPTH - 1) * 2 * G;

  unsigned ka[4], va[8];
#pragma unroll
  for (int kk = 0; kk < 4; ++kk) ka[kk] = c * 256 + 16 * ((g ^ c) ^ (4 * kk));
  {
    const unsigned qq = c >> 2, p = c & 3, row = 4 * g + qq;
#pragma unroll
    for (int dt = 0; dt < 8; ++dt) va[dt] = row * 256 + 32 * (dt ^ (row & 7)) + 8 * p;
  }

  bf16x8 qf[4];
  f32x4 acc[8];
#pragma unroll
  for (int dt = 0; dt < 8; ++dt) acc[dt] = f32x4{0.f, 0.f, 0.f, 0.f};
  float m = -INFINITY, l = 0.f;

  wait_vm<PWAIT>();
  if constexpr (C::STAMP) { unsigned t = (unsigned)__builtin_amdgcn_s_memrealtime(); asm volatile("s_waitcnt lgkmcnt(0)" ::: "memory"); pv = (lane == 17) ? t : pv; pv = (lane == 18) ? (unsigned)t0 : pv; }
  __builtin_amdgcn_s_barrier();
#pragma unroll
  for (int kk = 0; kk < 4; ++kk) qf[kk] = lds_b128(smem + C::LDS_Q + ka[kk]);

  if (w == NW - 1) {
    f32x4 s = {0.f, 0.f, 0.f, 0.f};
#pragma unroll
    for (int kk = 0; kk < 4; ++kk) s = __builtin_amdgcn_mfma_f32_16x16x32_bf16(lds_b128(smem + C::LDS_KT + ka[kk]), qf[kk], s, 0, 0, 0);
    const u32x2 mk = *(const u32x2 LAS*)(smem + C::LDS_MASK + c * 32 + g * 8);
    const float mv[4] = {__uint_as_float(mk[0] << 16), __uint_as_float(mk[0] & 0xffff0000u), __uint_as_float(mk[1] << 16), __uint_as_float(mk[1] & 0xffff0000u)};
    float t[4];
#pragma unroll
    for (int r = 0; r < 4; ++r) t[r] = (s[r] * kScale + (1.0f - mv[r]) * -1000000000.0f) * kL2E;
    const float mx = xg_max(fmaxf(fmaxf(t[0], t[1]), fmaxf(t[2], t[3])));
    m = mx;
    float p[4];
#pragma unroll
    for (int r = 0; r < 4; ++r) { p[r] = __builtin_amdgcn_exp2f(t[r] - mx); l += p[r]; }
    const u32x2 pw = {cvtpk(p[0], p[1]), cvtpk(p[2], p[3])};
    const s16x4 pf = __builtin_bit_cast(s16x4, pw);
#pragma unroll
    for (int dt = 0; dt < 8; ++dt) acc[dt] = __builtin_amdgcn_mfma_f32_16x16x16bf16_1k(lds_tr(smem + C::LDS_VT + va[dt]), pf, acc[dt], 0, 0, 0);
  }

  const char LAS* kring = smem + w * WREG;
  const char LAS* vring = kring + DEPTH * SLOT;

  auto step = [&](int s, auto slot_c, auto wk_c, auto wv_c, auto issue_c) {
    constexpr int slot = decltype(slot_c)::value, WK = decltype(wk_c)::value, WV = decltype(wv_c)::value;
    constexpr bool ISSUE = decltype(issue_c)::value;
    SB();
    wait_vm<WK>();
    if constexpr (C::STAMP) { if ((s % (NS / 16 > 0 ? NS / 16 : 1)) == 0) { unsigned t = (unsigned)__builtin_amdgcn_s_memrealtime(); asm volatile("s_waitcnt lgkmcnt(0)" ::: "memory"); pv = (lane == s / (NS / 16 > 0 ? NS / 16 : 1)) ? t : pv; } }
    unsigned tnow = 0;
    if constexpr (C::PACE && ISSUE) tnow = (unsigned)__builtin_amdgcn_s_memrealtime();
    bf16x8 kf[TK][4];
#pragma unroll
    for (int t = 0; t < TK; ++t)
#pragma unroll
      for (int kk = 0; kk < 4; ++kk) kf[t][kk] = lds_b128(kring + slot * SLOT + t * 4096 + ka[kk]);
    if constexpr (ISSUE) {
      wait_lgkm0();
      if constexpr (C::PACE) {
        const unsigned target = (unsigned)((s + 1) * C::PACE) / (unsigned)NS;
        while (tnow - (unsigned)t0 < target) { __builtin_amdgcn_s_sleep(1); tnow = (unsigned)__builtin_amdgcn_s_memrealtime(); }
      }
      issueK(s + DEPTH, slot);
    }
    SB();
    f32x4 sc[TK];
#pragma unroll
    for (int t = 0; t < TK; ++t) {
      sc[t] = f32x4{0.f, 0.f, 0.f, 0.f};
#pragma unroll
      for (int kk = 0; kk < 4; ++kk) sc[t] = __builtin_amdgcn_mfma_f32_16x16x32_bf16(kf[t][kk], qf[kk], sc[t], 0, 0, 0);
    }
    float mx = fmaxf(fmaxf(sc[0][0], sc[0][1]), fmaxf(sc[0][2], sc[0][3]));
    if constexpr (TK == 2) mx = fmaxf(mx, fmaxf(fmaxf(sc[1][0], sc[1][1]), fmaxf(sc[1][2], sc[1][3])));
    mx = xg_max(mx) * kC;
    float mn;
    if constexpr (C::CRS) {
      if (__builtin_amdgcn_ballot_w64(mx > m) != 0) {
        mn = fmaxf(m, mx);
        const float alpha = __builtin_amdgcn_exp2f(m - mn);
        m = mn;
        l *= alpha;
#pragma unroll
        for (int dt = 0; dt < 8; ++dt) acc[dt] *= alpha;
      }
      mn = m;
    }
    float alpha = 1.f;
    if constexpr (!C::CRS) {
      mn = fmaxf(m, mx);
      alpha = __builtin_amdgcn_exp2f(m - mn);
      m = mn;
    }
    float p[4 * TK];
    float ps = 0.f;
#pragma unroll
    for (int t = 0; t < TK; ++t)
#pragma unroll
      for (int r = 0; r < 4; ++r) { p[4 * t + r] = __builtin_amdgcn_exp2f(fmaf(sc[t][r], kC, -mn)); ps += p[4 * t + r]; }
    if constexpr (C::CRS) l += ps; else l = fmaf(l, alpha, ps);
    if constexpr (!C::CRS) {
#pragma unroll
      for (int dt = 0; dt < 8; ++dt) acc[dt] *= alpha;
    }
    unsigned pw[2 * TK];
#pragma unroll
    for (int i = 0; i < 2 * TK; ++i) pw[i] = cvtpk(p[2 * i], p[2 * i + 1]);
    SB();
    wait_vm<WV>();
    s16x4 vf[8][TK];
#pragma unroll
    for (int dt = 0; dt < 8; ++dt)
#pragma unroll
      for (int t = 0; t < TK; ++t) vf[dt][t] = lds_tr(vring + slot * SLOT + t * 4096 + va[dt]);
    if constexpr (ISSUE) { wait_lgkm0(); issueV(s + DEPTH, slot); }
    SB();
    if constexpr (TK == 2) {
      const u32x4 pq = {pw[0], pw[1], pw[2], pw[3]};
      const bf16x8 pf = __builtin_bit_cast(bf16x8, pq);
#pragma unroll
      for (int dt = 0; dt < 8; ++dt) {
        const s16x8 a = {vf[dt][0][0], vf[dt][0][1], vf[dt][0][2], vf[dt][0][3], vf[dt][1][0], vf[dt][1][1], vf[dt][1][2], vf[dt][1][3]};
        acc[dt] = __builtin_amdgcn_mfma_f32_16x16x32_bf16(__builtin_bit_cast(bf16x8, a), pf, acc[dt], 0, 0, 0);
      }
    } else {
      const u32x2 pq = {pw[0], pw[1]};
      const s16x4 pf = __builtin_bit_cast(s16x4, pq);
#pragma unroll
      for (int dt = 0; dt < 8; ++dt) acc[dt] = __builtin_amdgcn_mfma_f32_16x16x16bf16_1k(vf[dt][0], pf, acc[dt], 0, 0, 0);
    }
  };
  using std::integral_constant;
  constexpr int WS = G * (2 * DEPTH - 1);
  constexpr int R = (NS - DEPTH) / DEPTH;
  for (int r = 0; r < R; ++r)
    sfor<DEPTH>([&](auto i) { step(r * DEPTH + i.value, i, integral_constant<int, WS>{}, integral_constant<int, WS>{}, integral_constant<bool, true>{}); });
  sfor<NS - R * DEPTH>([&](auto i) {
    constexpr int s = R * DEPTH + i.value;
    constexpr int last = (s + DEPTH - 1 < NS - 1) ? s + DEPTH - 1 : NS - 1;
    constexpr bool iss = s + DEPTH < NS;
    step(s, integral_constant<int, s % DEPTH>{}, integral_constant<int, G * (1 + 2 * (last - s))>{},
         integral_constant<int, G * (2 * (last - s) + (iss ? 1 : 0))>{}, integral_constant<bool, iss>{});
  });

  if constexpr (C::STAMP) { unsigned t = (unsigned)__builtin_amdgcn_s_memrealtime(); asm volatile("s_waitcnt lgkmcnt(0)" ::: "memory"); pv = (lane == 16) ? t : pv; }
  l = xg_sum(l);
  {
    char LAS* mr = smem + w * WREG;
#pragma unroll
    for (int dt = 0; dt < 8; ++dt) *(f32x4 LAS*)(mr + c * MROW + (16 * dt + 4 * g) * 4) = acc[dt];
    if (g == 0) { *(float LAS*)(mr + 16 * MROW + c * 8) = m; *(float LAS*)(mr + 16 * MROW + c * 8 + 4) = l; }
  }
  __syncthreads();
  if (tid < 256) {
    const int qi = tid >> 4, dc = tid & 15;
    float mw[NW], lw[NW], M = -INFINITY;
#pragma unroll
    for (int x = 0; x < NW; ++x) {
      mw[x] = *(const float LAS*)(smem + x * WREG + 16 * MROW + qi * 8);
      lw[x] = *(const float LAS*)(smem + x * WREG + 16 * MROW + qi * 8 + 4);
      M = fmaxf(M, mw[x]);
    }
    float L = 0.f;
    f32x4 o0 = {0.f, 0.f, 0.f, 0.f}, o1 = {0.f, 0.f, 0.f, 0.f};
#pragma unroll
    for (int x = 0; x < NW; ++x) {
      const float f = __builtin_amdgcn_exp2f(mw[x] - M);
      L = fmaf(f, lw[x], L);
      const f32x4 a0 = *(const f32x4 LAS*)(smem + x * WREG + qi * MROW + dc * 32);
      const f32x4 a1 = *(const f32x4 LAS*)(smem + x * WREG + qi * MROW + dc * 32 + 16);
      o0 += f * a0; o1 += f * a1;
    }
    const float inv = 1.0f / L;
    const u32x4 ow = {cvtpk(o0[0] * inv, o0[1] * inv), cvtpk(o0[2] * inv, o0[3] * inv), cvtpk(o1[0] * inv, o1[1] * inv), cvtpk(o1[2] * inv, o1[3] * inv)};
    u32x4* op = (u32x4*)(out + ((size_t)bh * T_ + qi) * D_ + dc * 8);
    if constexpr (C::OST == 1) __builtin_nontemporal_store(ow, op);
    else if constexpr (C::OST == 2) asm volatile("global_store_dwordx4 %0, %1, off sc0 sc1\n\ts_nop 1" :: "v"(op), "v"(ow) : "memory");
    else *op = ow;
  }
  if constexpr (C::STAMP) {
    unsigned long long t1 = __builtin_amdgcn_s_memrealtime();
    unsigned xcc; asm volatile("s_getreg_b32 %0, hwreg(HW_REG_XCC_ID)" : "=s"(xcc));
    if (tid == 0) { stamps[2 * bh] = t0; stamps[2 * bh + 1] = t1; stamps[8192 + bh] = xcc & 0xf; }
    if (w == 0 && lane < 32) stamps[16384 + bh * 32 + lane] = pv;
  }
}

}

extern "C" void kernel_launch(void* const* d_in, const int* in_sizes, int n_in, void* d_out, int out_size, void* d_ws, size_t ws_size,
                              hipStream_t stream) {
  (void)in_sizes; (void)n_in; (void)out_size; (void)ws_size;
  using C = ta::Cfg<4, 1, 2, 0, 1, 0, 0, 7500, 0, 0, 0>;
  (void)hipFuncSetAttribute((const void*)ta::tree_attn<C>, hipFuncAttributeMaxDynamicSharedMemorySize, C::LDS_TOTAL);
  hipLaunchKernelGGL((ta::tree_attn<C>), dim3(256), dim3(C::NW * 64), C::LDS_TOTAL, stream,
                     (const uint16_t*)d_in[0], (const uint16_t*)d_in[1], (const uint16_t*)d_in[2], (const uint16_t*)d_in[3],
                     (const uint16_t*)d_in[4], (const uint16_t*)d_in[5], (uint16_t*)d_out, (unsigned long long*)d_ws);
}
```

```cpp
#include <hip/hip_runtime.h>
#include <hip/hip_bf16.h>
#include <stdint.h>
#include <utility>

namespace ta {

constexpr int B_ = 8, H_ = 32, T_ = 16, C_ = 4096, D_ = 128;
constexpr int MROW = 528;

typedef __attribute__((ext_vector_type(8))) __bf16 bf16x8;
typedef __attribute__((ext_vector_type(4))) short s16x4;
typedef __attribute__((ext_vector_type(8))) short s16x8;
typedef __attribute__((ext_vector_type(4))) float f32x4;
typedef __attribute__((ext_vector_type(4))) unsigned u32x4;
typedef __attribute__((ext_vector_type(2))) unsigned u32x2;
#define LAS __attribute__((address_space(3)))
#define SB() __builtin_amdgcn_sched_barrier(0)

__device__ __forceinline__ unsigned cvtpk(float lo, float hi) {
  typedef __attribute__((ext_vector_type(2))) __bf16 bf16x2;
  typedef __attribute__((ext_vector_type(2))) float f32x2;
  f32x2 v = {lo, hi};
  bf16x2 r = __builtin_convertvector(v, bf16x2);
  return __builtin_bit_cast(unsigned, r);
}
__device__ __forceinline__ float xg_max(float x) {
  auto s = __builtin_amdgcn_permlane16_swap(__float_as_uint(x), __float_as_uint(x), false, false);
  x = fmaxf(__uint_as_float(s[0]), __uint_as_float(s[1]));
  auto t = __builtin_amdgcn_permlane32_swap(__float_as_uint(x), __float_as_uint(x), false, false);
  return fmaxf(__uint_as_float(t[0]), __uint_as_float(t[1]));
}
__device__ __forceinline__ float xg_sum(float x) {
  auto s = __builtin_amdgcn_permlane16_swap(__float_as_uint(x), __float_as_uint(x), false, false);
  x = __uint_as_float(s[0]) + __uint_as_float(s[1]);
  auto t = __builtin_amdgcn_permlane32_swap(__float_as_uint(x), __float_as_uint(x), false, false);
  return __uint_as_float(t[0]) + __uint_as_float(t[1]);
}

#define DMA4(POL) asm volatile( \
      "s_mov_b32 m0, %0\n\t" \
      "s_nop 0\n\t" \
      "buffer_load_dwordx4 %1, %5, %6 offen" POL " lds\n\t" \
      "buffer_load_dwordx4 %2, %5, %6 offen offset:1024" POL " lds\n\t" \
      "buffer_load_dwordx4 %3, %5, %6 offen offset:2048" POL " lds\n\t" \
      "buffer_load_dwordx4 %4, %5, %6 offen offset:3072" POL " lds" \
      :: "s"(m0v), "v"(v0), "v"(v1), "v"(v2), "v"(v3), "s"(srd), "s"(soff) : "memory")
template <int NT>
__device__ __forceinline__ void dma_tile(unsigned m0v, unsigned v0, unsigned v1, unsigned v2, unsigned v3,
                                         __amdgpu_buffer_rsrc_t srd, unsigned soff) {
  if constexpr (NT == 1) DMA4(" nt");
  else if constexpr (NT == 4) DMA4(" nt sc1");
  else if constexpr (NT == 5) DMA4(" sc0 nt sc1");
  else if constexpr (NT == 6) DMA4(" sc1");
  else DMA4("");
}
template <int N> __device__ __forceinline__ void wait_vm() { static_assert(N >= 0 && N <= 63, "vmcnt"); asm volatile("s_waitcnt vmcnt(%0)" :: "i"(N) : "memory"); }
__device__ __forceinline__ void wait_lgkm0() { asm volatile("s_waitcnt lgkmcnt(0)" ::: "memory"); }

constexpr float kScale = 0.088388347648318440f;
constexpr float kL2E = 1.4426950408889634f;
constexpr float kC = kScale * kL2E;

__device__ __forceinline__ bf16x8 lds_b128(const char LAS* p) { return *(const bf16x8 LAS*)p; }
__device__ __forceinline__ s16x4 lds_tr(const char LAS* p) { return __builtin_amdgcn_ds_read_tr16_b64_v4i16((s16x4 LAS*)p); }

template <class F, int... I> __device__ __forceinline__ void sfor_impl(F&& f, std::integer_sequence<int, I...>) { (f(std::integral_constant<int, I>{}), ...); }
template <int N, class F> __device__ __forceinline__ void sfor(F&& f) { sfor_impl(f, std::make_integer_sequence<int, N>{}); }

template <int NW_, int TK_, int DEPTH_, int ORD_ = 0, int NT_ = 1, int STAMP_ = 0, int CRS_ = 0, int PACE_ = 0, int PRO_ = 0, int TOUCH_ = 0, int OST_ = 0>
struct Cfg {
  static constexpr int NW = NW_, TK = TK_, DEPTH = DEPTH_, ORD = ORD_, NT = NT_, STAMP = STAMP_, CRS = CRS_, PACE = PACE_, PRO = PRO_, TOUCH = TOUCH_, OST = OST_;
  static constexpr int SLOT = TK * 4096;
  static constexpr int RING = DEPTH * 2 * SLOT;
  static constexpr int NS = C_ / (16 * TK) / NW;
  static constexpr int MRG = (16 * MROW + 128 + 255) / 256 * 256;
  static constexpr int WREG = RING > MRG ? RING : MRG;
  static constexpr int LDS_Q = NW * WREG, LDS_KT = LDS_Q + 4096, LDS_VT = LDS_KT + 4096, LDS_MASK = LDS_VT + 4096;
  static constexpr int LDS_DUMMY = LDS_MASK + 1024;
  static constexpr int LDS_USED = LDS_DUMMY + 1024;
  static constexpr int LDS_TOTAL = LDS_USED > 83968 ? LDS_USED : 83968;
  static_assert(LDS_TOTAL <= 163840, "LDS");
  static_assert(DEPTH * 2 * TK * 4 <= 63 - 16 / NW + 0 || DEPTH * 2 * TK * 4 <= 63, "vmcnt range");
};

template <class C>
__global__ __launch_bounds__(C::NW * 64, C::NW >= 16 ? 4 : 2) void tree_attn(
    const uint16_t* __restrict__ q, const uint16_t* __restrict__ kc, const uint16_t* __restrict__ vc,
    const uint16_t* __restrict__ kt, const uint16_t* __restrict__ vt, const uint16_t* __restrict__ mask,
    uint16_t* __restrict__ out, unsigned long long* __restrict__ stamps) {
  constexpr int NW = C::NW, TK = C::TK, DEPTH = C::DEPTH, NS = C::NS, SLOT = C::SLOT, WREG = C::WREG, G = 4 * TK;
  extern __shared__ __attribute__((aligned(16))) char smem_[];
  char LAS* smem = (char LAS*)smem_;
  const int tid = threadIdx.x, lane = tid & 63;
  const int w = __builtin_amdgcn_readfirstlane(tid >> 6);
  const int bh = blockIdx.x, b = bh >> 5;
  const unsigned g = lane >> 4, c = lane & 15;
  const unsigned lds0 = (unsigned)(size_t)smem;
  unsigned long long t0 = 0; unsigned pv = 0;
  if constexpr (C::STAMP || C::PACE) t0 = __builtin_amdgcn_s_memrealtime();

  const __amdgpu_buffer_rsrc_t srdK = __builtin_amdgcn_make_buffer_rsrc((void*)(kc + (size_t)bh * C_ * D_), 0, C_ * D_ * 2, 0x00020000);
  const __amdgpu_buffer_rsrc_t srdV = __builtin_amdgcn_make_buffer_rsrc((void*)(vc + (size_t)bh * C_ * D_), 0, C_ * D_ * 2, 0x00020000);

  unsigned vk[4], vv[2];
#pragma unroll
  for (int i = 0; i < 4; ++i) vk[i] = g * 256 + 16 * ((c ^ g) ^ (4 * i));
#pragma unroll
  for (int j = 0; j < 2; ++j) vv[j] = g * 256 + 16 * (c ^ (2 * g) ^ (8 * j));

  const unsigned ring = lds0 + w * WREG;
  auto step_off = [&](int s) -> unsigned { return C::ORD == 0 ? (unsigned)(s * NW + w) * SLOT : (unsigned)(w * NS + s) * SLOT; };
  auto issueK = [&](int s, int slot) {
    const unsigned o = step_off(s);
#pragma unroll
    for (int t = 0; t < TK; ++t) dma_tile<(C::NT >= 4 ? C::NT : (C::NT == 1 || C::NT == 2))>(ring + slot * SLOT + t * 4096, vk[0], vk[1], vk[2], vk[3], srdK, o + t * 4096);
  };
  auto issueV = [&](int s, int slot) {
    const unsigned o = step_off(s);
#pragma unroll
    for (int t = 0; t < TK; ++t) dma_tile<(C::NT >= 4 ? C::NT : (C::NT == 1 || C::NT == 3))>(ring + DEPTH * SLOT + slot * SLOT + t * 4096, vv[0], vv[1], vv[0], vv[1], srdV, o + t * 4096);
  };

  constexpr int PPW = NW <= 16 ? 16 / NW : 1;
  auto pieces = [&]() {
    const size_t tb = (size_t)bh * T_ * D_ * 2;
#pragma unroll
    for (int j = 0; j < PPW; ++j) {
      const int pi = (w * PPW + j) & 15;
      const int kind = pi & 3, rg = pi >> 2;
      const unsigned row = 4 * rg + g;
      const char* src; unsigned dst;
      if (kind == 0)      { src = (const char*)q + tb + row * 256 + 16 * (c ^ row);              dst = C::LDS_Q + rg * 1024; }
      else if (kind == 1) { src = (const char*)kt + tb + row * 256 + 16 * (c ^ row);             dst = C::LDS_KT + rg * 1024; }
      else if (kind == 2) { src = (const char*)vt + tb + row * 256 + 16 * (c ^ (2 * (row & 7))); dst = C::LDS_VT + rg * 1024; }
      else                { src = (const char*)mask + (size_t)b * 512 + (lane & 31) * 16;        dst = C::LDS_MASK; }
      const unsigned m0v = __builtin_amdgcn_readfirstlane(lds0 + dst);
      asm volatile("s_nop 4\n\ts_mov_b32 m0, %0\n\ts_nop 0\n\tglobal_load_lds_dwordx4 %1, off" :: "s"(m0v), "v"(src) : "memory");
    }
    if constexpr (C::TOUCH) {
      const char* src = (const char*)out + tb + lane * 16;
      const unsigned m0v = lds0 + C::LDS_DUMMY;
      asm volatile("s_mov_b32 m0, %0\n\ts_nop 0\n\tglobal_load_lds_dwordx4 %1, off" :: "s"(m0v), "v"(src) : "memory");
    }
  };
  if constexpr (C::PRO == 0) {
    pieces();
#pragma unroll
    for (int s = 0; s < DEPTH; ++s) { issueK(s, s); issueV(s, s); }
  } else {
    issueK(0, 0); issueV(0, 0);
    pieces();
#pragma unroll
    for (int s = 1; s < DEPTH; ++s) { issueK(s, s); issueV(s, s); }
  }
  constexpr int PWAIT = C::PRO == 0 ? DEPTH * 2 * G : (DEPTH - 1) * 2 * G;

  unsigned ka[4], va[8];
#pragma unroll
  for (int kk = 0; kk < 4; ++kk) ka[kk] = c * 256 + 16 * ((g ^ c) ^ (4 * kk));
  {
    const unsigned qq = c >> 2, p = c & 3, row = 4 * g + qq;
#pragma unroll
    for (int dt = 0; dt < 8; ++dt) va[dt] = row * 256 + 32 * (dt ^ (row & 7)) + 8 * p;
  }

  bf16x8 qf[4];
  f32x4 acc[8];
#pragma unroll
  for (int dt = 0; dt < 8; ++dt) acc[dt] = f32x4{0.f, 0.f, 0.f, 0.f};
  float m = -INFINITY, l = 0.f;

  wait_vm<PWAIT>();
  if constexpr (C::STAMP) { unsigned t = (unsigned)__builtin_amdgcn_s_memrealtime(); asm volatile("s_waitcnt lgkmcnt(0)" ::: "memory"); pv = (lane == 17) ? t : pv; pv = (lane == 18) ? (unsigned)t0 : pv; }
  __builtin_amdgcn_s_barrier();
#pragma unroll
  for (int kk = 0; kk < 4; ++kk) qf[kk] = lds_b128(smem + C::LDS_Q + ka[kk]);

  if (w == NW - 1) {
    f32x4 s = {0.f, 0.f, 0.f, 0.f};
#pragma unroll
    for (int kk = 0; kk < 4; ++kk) s = __builtin_amdgcn_mfma_f32_16x16x32_bf16(lds_b128(smem + C::LDS_KT + ka[kk]), qf[kk], s, 0, 0, 0);
    const u32x2 mk = *(const u32x2 LAS*)(smem + C::LDS_MASK + c * 32 + g * 8);
    const float mv[4] = {__uint_as_float(mk[0] << 16), __uint_as_float(mk[0] & 0xffff0000u), __uint_as_float(mk[1] << 16), __uint_as_float(mk[1] & 0xffff0000u)};
    float t[4];
#pragma unroll
    for (int r = 0; r < 4; ++r) t[r] = (s[r] * kScale + (1.0f - mv[r]) * -1000000000.0f) * kL2E;
    const float mx = xg_max(fmaxf(fmaxf(t[0], t[1]), fmaxf(t[2], t[3])));
    m = mx;
    float p[4];
#pragma unroll
    for (int r = 0; r < 4; ++r) { p[r] = __builtin_amdgcn_exp2f(t[r] - mx); l += p[r]; }
    const u32x2 pw = {cvtpk(p[0], p[1]), cvtpk(p[2], p[3])};
    const s16x4 pf = __builtin_bit_cast(s16x4, pw);
#pragma unroll
    for (int dt = 0; dt < 8; ++dt) acc[dt] = __builtin_amdgcn_mfma_f32_16x16x16bf16_1k(lds_tr(smem + C::LDS_VT + va[dt]), pf, acc[dt], 0, 0, 0);
  }

  const char LAS* kring = smem + w * WREG;
  const char LAS* vring = kring + DEPTH * SLOT;

  auto step = [&](int s, auto slot_c, auto wk_c, auto wv_c, auto issue_c) {
    constexpr int slot = decltype(slot_c)::value, WK = decltype(wk_c)::value, WV = decltype(wv_c)::value;
    constexpr bool ISSUE = decltype(issue_c)::value;
    SB();
    wait_vm<WK>();
    if constexpr (C::STAMP) { if ((s % (NS / 16 > 0 ? NS / 16 : 1)) == 0) { unsigned t = (unsigned)__builtin_amdgcn_s_memrealtime(); asm volatile("s_waitcnt lgkmcnt(0)" ::: "memory"); pv = (lane == s / (NS / 16 > 0 ? NS / 16 : 1)) ? t : pv; } }
    unsigned tnow = 0;
    if constexpr (C::PACE && ISSUE) tnow = (unsigned)__builtin_amdgcn_s_memrealtime();
    bf16x8 kf[TK][4];
#pragma unroll
    for (int t = 0; t < TK; ++t)
#pragma unroll
      for (int kk = 0; kk < 4; ++kk) kf[t][kk] = lds_b128(kring + slot * SLOT + t * 4096 + ka[kk]);
    if constexpr (ISSUE) {
      wait_lgkm0();
      if constexpr (C::PACE) {
        const unsigned target = (unsigned)((s + 1) * C::PACE) / (unsigned)NS;
        while (tnow - (unsigned)t0 < target) { __builtin_amdgcn_s_sleep(1); tnow = (unsigned)__builtin_amdgcn_s_memrealtime(); }
      }
      issueK(s + DEPTH, slot);
    }
    SB();
    f32x4 sc[TK];
#pragma unroll
    for (int t = 0; t < TK; ++t) {
      sc[t] = f32x4{0.f, 0.f, 0.f, 0.f};
#pragma unroll
      for (int kk = 0; kk < 4; ++kk) sc[t] = __builtin_amdgcn_mfma_f32_16x16x32_bf16(kf[t][kk], qf[kk], sc[t], 0, 0, 0);
    }
    float mx = fmaxf(fmaxf(sc[0][0], sc[0][1]), fmaxf(sc[0][2], sc[0][3]));
    if constexpr (TK == 2) mx = fmaxf(mx, fmaxf(fmaxf(sc[1][0], sc[1][1]), fmaxf(sc[1][2], sc[1][3])));
    mx = xg_max(mx) * kC;
    float mn;
    if constexpr (C::CRS) {
      if (__builtin_amdgcn_ballot_w64(mx > m) != 0) {
        mn = fmaxf(m, mx);
        const float alpha = __builtin_amdgcn_exp2f(m - mn);
        m = mn;
        l *= alpha;
#pragma unroll
        for (int dt = 0; dt < 8; ++dt) acc[dt] *= alpha;
      }
      mn = m;
    }
    float alpha = 1.f;
    if constexpr (!C::CRS) {
      mn = fmaxf(m, mx);
      alpha = __builtin_amdgcn_exp2f(m - mn);
      m = mn;
    }
    float p[4 * TK];
    float ps = 0.f;
#pragma unroll
    for (int t = 0; t < TK; ++t)
#pragma unroll
      for (int r = 0; r < 4; ++r) { p[4 * t + r] = __builtin_amdgcn_exp2f(fmaf(sc[t][r], kC, -mn)); ps += p[4 * t + r]; }
    if constexpr (C::CRS) l += ps; else l = fmaf(l, alpha, ps);
    if constexpr (!C::CRS) {
#pragma unroll
      for (int dt = 0; dt < 8; ++dt) acc[dt] *= alpha;
    }
    unsigned pw[2 * TK];
#pragma unroll
    for (int i = 0; i < 2 * TK; ++i) pw[i] = cvtpk(p[2 * i], p[2 * i + 1]);
    SB();
    wait_vm<WV>();
    s16x4 vf[8][TK];
#pragma unroll
    for (int dt = 0; dt < 8; ++dt)
#pragma unroll
      for (int t = 0; t < TK; ++t) vf[dt][t] = lds_tr(vring + slot * SLOT + t * 4096 + va[dt]);
    if constexpr (ISSUE) { wait_lgkm0(); issueV(s + DEPTH, slot); }
    SB();
    if constexpr (TK == 2) {
      const u32x4 pq = {pw[0], pw[1], pw[2], pw[3]};
      const bf16x8 pf = __builtin_bit_cast(bf16x8, pq);
#pragma unroll
      for (int dt = 0; dt < 8; ++dt) {
        const s16x8 a = {vf[dt][0][0], vf[dt][0][1], vf[dt][0][2], vf[dt][0][3], vf[dt][1][0], vf[dt][1][1], vf[dt][1][2], vf[dt][1][3]};
        acc[dt] = __builtin_amdgcn_mfma_f32_16x16x32_bf16(__builtin_bit_cast(bf16x8, a), pf, acc[dt], 0, 0, 0);
      }
    } else {
      const u32x2 pq = {pw[0], pw[1]};
      const s16x4 pf = __builtin_bit_cast(s16x4, pq);
#pragma unroll
      for (int dt = 0; dt < 8; ++dt) acc[dt] = __builtin_amdgcn_mfma_f32_16x16x16bf16_1k(vf[dt][0], pf, acc[dt], 0, 0, 0);
    }
  };
  using std::integral_constant;
  constexpr int WS = G * (2 * DEPTH - 1);
  constexpr int R = (NS - DEPTH) / DEPTH;
  for (int r = 0; r < R; ++r)
    sfor<DEPTH>([&](auto i) { step(r * DEPTH + i.value, i, integral_constant<int, WS>{}, integral_constant<int, WS>{}, integral_constant<bool, true>{}); });
  sfor<NS - R * DEPTH>([&](auto i) {
    constexpr int s = R * DEPTH + i.value;
    constexpr int last = (s + DEPTH - 1 < NS - 1) ? s + DEPTH - 1 : NS - 1;
    constexpr bool iss = s + DEPTH < NS;
    step(s, integral_constant<int, s % DEPTH>{}, integral_constant<int, G * (1 + 2 * (last - s))>{},
         integral_constant<int, G * (2 * (last - s) + (iss ? 1 : 0))>{}, integral_constant<bool, iss>{});
  });

  if constexpr (C::STAMP) { unsigned t = (unsigned)__builtin_amdgcn_s_memrealtime(); asm volatile("s_waitcnt lgkmcnt(0)" ::: "memory"); pv = (lane == 16) ? t : pv; }
  l = xg_sum(l);
  {
    char LAS* mr = smem + w * WREG;
#pragma unroll
    for (int dt = 0; dt < 8; ++dt) *(f32x4 LAS*)(mr + c * MROW + (16 * dt + 4 * g) * 4) = acc[dt];
    if (g == 0) { *(float LAS*)(mr + 16 * MROW + c * 8) = m; *(float LAS*)(mr + 16 * MROW + c * 8 + 4) = l; }
  }
  __syncthreads();
  if (tid < 256) {
    const int qi = tid >> 4, dc = tid & 15;
    float mw[NW], lw[NW], M = -INFINITY;
#pragma unroll
    for (int x = 0; x < NW; ++x) {
      mw[x] = *(const float LAS*)(smem + x * WREG + 16 * MROW + qi * 8);
      lw[x] = *(const float LAS*)(smem + x * WREG + 16 * MROW + qi * 8 + 4);
      M = fmaxf(M, mw[x]);
    }
    float L = 0.f;
    f32x4 o0 = {0.f, 0.f, 0.f, 0.f}, o1 = {0.f, 0.f, 0.f, 0.f};
#pragma unroll
    for (int x = 0; x < NW; ++x) {
      const float f = __builtin_amdgcn_exp2f(mw[x] - M);
      L = fmaf(f, lw[x], L);
      const f32x4 a0 = *(const f32x4 LAS*)(smem + x * WREG + qi * MROW + dc * 32);
      const f32x4 a1 = *(const f32x4 LAS*)(smem + x * WREG + qi * MROW + dc * 32 + 16);
      o0 += f * a0; o1 += f * a1;
    }
    const float inv = 1.0f / L;
    const u32x4 ow = {cvtpk(o0[0] * inv, o0[1] * inv), cvtpk(o0[2] * inv, o0[3] * inv), cvtpk(o1[0] * inv, o1[1] * inv), cvtpk(o1[2] * inv, o1[3] * inv)};
    u32x4* op = (u32x4*)(out + ((size_t)bh * T_ + qi) * D_ + dc * 8);
    if constexpr (C::OST == 1) __builtin_nontemporal_store(ow, op);
    else if constexpr (C::OST == 2) asm volatile("global_store_dwordx4 %0, %1, off sc0 sc1\n\ts_nop 1" :: "v"(op), "v"(ow) : "memory");
    else *op = ow;
  }
  if constexpr (C::STAMP) {
    unsigned long long t1 = __builtin_amdgcn_s_memrealtime();
    unsigned xcc; asm volatile("s_getreg_b32 %0, hwreg(HW_REG_XCC_ID)" : "=s"(xcc));
    if (tid == 0) { stamps[2 * bh] = t0; stamps[2 * bh + 1] = t1; stamps[8192 + bh] = xcc & 0xf; }
    if (w == 0 && lane < 32) stamps[16384 + bh * 32 + lane] = pv;
  }
}

}

#define NTHR(C) (C::NW * 64)

extern "C" void kernel_launch(void* const* d_in, const int* in_sizes, int n_in, void* d_out, int out_size, void* d_ws, size_t ws_size,
                              hipStream_t stream) {
  (void)in_sizes; (void)n_in; (void)out_size; (void)ws_size;
  using C = ta::Cfg<4, 1, 4, 0, 1, 0, 0, 9600, 0, 0, 0>;
  (void)hipFuncSetAttribute((const void*)ta::tree_attn<C>, hipFuncAttributeMaxDynamicSharedMemorySize, C::LDS_TOTAL);
  hipLaunchKernelGGL((ta::tree_attn<C>), dim3(256), dim3(NTHR(C)), C::LDS_TOTAL, stream,
                     (const uint16_t*)d_in[0], (const uint16_t*)d_in[1], (const uint16_t*)d_in[2], (const uint16_t*)d_in[3],
                     (const uint16_t*)d_in[4], (const uint16_t*)d_in[5], (uint16_t*)d_out, (unsigned long long*)d_ws);
}
```

```cpp
#include <hip/hip_runtime.h>
#include <hip/hip_bf16.h>
#include <stdint.h>
#include <utility>

namespace ta {

constexpr int B_ = 8, H_ = 32, T_ = 16, C_ = 4096, D_ = 128;
constexpr int MROW = 528;

typedef __attribute__((ext_vector_type(8))) __bf16 bf16x8;
typedef __attribute__((ext_vector_type(4))) short s16x4;
typedef __attribute__((ext_vector_type(8))) short s16x8;
typedef __attribute__((ext_vector_type(4))) float f32x4;
typedef __attribute__((ext_vector_type(4))) unsigned u32x4;
typedef __attribute__((ext_vector_type(2))) unsigned u32x2;
#define LAS __attribute__((address_space(3)))
#define SB() __builtin_amdgcn_sched_barrier(0)

__device__ __forceinline__ unsigned cvtpk(float lo, float hi) {
  typedef __attribute__((ext_vector_type(2))) __bf16 bf16x2;
  typedef __attribute__((ext_vector_type(2))) float f32x2;
  f32x2 v = {lo, hi};
  bf16x2 r = __builtin_convertvector(v, bf16x2);
  return __builtin_bit_cast(unsigned, r);
}
__device__ __forceinline__ float xg_max(float x) {
  auto s = __builtin_amdgcn_permlane16_swap(__float_as_uint(x), __float_as_uint(x), false, false);
  x = fmaxf(__uint_as_float(s[0]), __uint_as_float(s[1]));
  auto t = __builtin_amdgcn_permlane32_swap(__float_as_uint(x), __float_as_uint(x), false, false);
  return fmaxf(__uint_as_float(t[0]), __uint_as_float(t[1]));
}
__device__ __forceinline__ float xg_sum(float x) {
  auto s = __builtin_amdgcn_permlane16_swap(__float_as_uint(x), __float_as_uint(x), false, false);
  x = __uint_as_float(s[0]) + __uint_as_float(s[1]);
  auto t = __builtin_amdgcn_permlane32_swap(__float_as_uint(x), __float_as_uint(x), false, false);
  return __uint_as_float(t[0]) + __uint_as_float(t[1]);
}

#define DMA4(POL) asm volatile( \
      "s_mov_b32 m0, %0\n\t" \
      "s_nop 0\n\t" \
      "buffer_load_dwordx4 %1, %5, %6 offen" POL " lds\n\t" \
      "buffer_load_dwordx4 %2, %5, %6 offen offset:1024" POL " lds\n\t" \
      "buffer_load_dwordx4 %3, %5, %6 offen offset:2048" POL " lds\n\t" \
      "buffer_load_dwordx4 %4, %5, %6 offen offset:3072" POL " lds" \
      :: "s"(m0v), "v"(v0), "v"(v1), "v"(v2), "v"(v3), "s"(srd), "s"(soff) : "memory")
template <int NT>
__device__ __forceinline__ void dma_tile(unsigned m0v, unsigned v0, unsigned v1, unsigned v2, unsigned v3,
                                         __amdgpu_buffer_rsrc_t srd, unsigned soff) {
  if constexpr (NT == 1) DMA4(" nt");
  else if constexpr (NT == 4) DMA4(" nt sc1");
  else if constexpr (NT == 5) DMA4(" sc0 nt sc1");
  else if constexpr (NT == 6) DMA4(" sc1");
  else DMA4("");
}
template <int N> __device__ __forceinline__ void wait_vm() { static_assert(N >= 0 && N <= 63, "vmcnt"); asm volatile("s_waitcnt vmcnt(%0)" :: "i"(N) : "memory"); }
__device__ __forceinline__ void wait_lgkm0() { asm volatile("s_waitcnt lgkmcnt(0)" ::: "memory"); }

constexpr float kScale = 0.088388347648318440f;
constexpr float kL2E = 1.4426950408889634f;
constexpr float kC = kScale * kL2E;

__device__ __forceinline__ bf16x8 lds_b128(const char LAS* p) { return *(const bf16x8 LAS*)p; }
__device__ __forceinline__ s16x4 lds_tr(const char LAS* p) { return __builtin_amdgcn_ds_read_tr16_b64_v4i16((s16x4 LAS*)p); }

template <class F, int... I> __device__ __forceinline__ void sfor_impl(F&& f, std::integer_sequence<int, I...>) { (f(std::integral_constant<int, I>{}), ...); }
template <int N, class F> __device__ __forceinline__ void sfor(F&& f) { sfor_impl(f, std::make_integer_sequence<int, N>{}); }

template <int NW_, int TK_, int DEPTH_, int ORD_ = 0, int NT_ = 1, int STAMP_ = 0, int CRS_ = 0, int PACE_ = 0, int PRO_ = 0, int TOUCH_ = 0, int OST_ = 0, int STG_ = 0, int XONLY_ = -1>
struct Cfg {
  static constexpr int NW = NW_, TK = TK_, DEPTH = DEPTH_, ORD = ORD_, NT = NT_, STAMP = STAMP_, CRS = CRS_, PACE = PACE_, PRO = PRO_, TOUCH = TOUCH_, OST = OST_, STG = STG_, XONLY = XONLY_;
  static constexpr int SLOT = TK * 4096;
  static constexpr int RING = DEPTH * 2 * SLOT;
  static constexpr int NS = C_ / (16 * TK) / NW;
  static constexpr int MRG = (16 * MROW + 128 + 255) / 256 * 256;
  static constexpr int WREG = RING > MRG ? RING : MRG;
  static constexpr int LDS_Q = NW * WREG, LDS_KT = LDS_Q + 4096, LDS_VT = LDS_KT + 4096, LDS_MASK = LDS_VT + 4096;
  static constexpr int LDS_DUMMY = LDS_MASK + 1024;
  static constexpr int LDS_USED = LDS_DUMMY + 1024;
  static constexpr int LDS_TOTAL = LDS_USED > 83968 ? LDS_USED : 83968;
  static_assert(LDS_TOTAL <= 163840, "LDS");
  static_assert(DEPTH * 2 * TK * 4 <= 63 - 16 / NW + 0 || DEPTH * 2 * TK * 4 <= 63, "vmcnt range");
};

template <class C>
__global__ __launch_bounds__(C::NW * 64, C::NW >= 16 ? 4 : 2) void tree_attn(
    const uint16_t* __restrict__ q, const uint16_t* __restrict__ kc, const uint16_t* __restrict__ vc,
    const uint16_t* __restrict__ kt, const uint16_t* __restrict__ vt, const uint16_t* __restrict__ mask,
    uint16_t* __restrict__ out, unsigned long long* __restrict__ stamps) {
  constexpr int NW = C::NW, TK = C::TK, DEPTH = C::DEPTH, NS = C::NS, SLOT = C::SLOT, WREG = C::WREG, G = 4 * TK;
  extern __shared__ __attribute__((aligned(16))) char smem_[];
  char LAS* smem = (char LAS*)smem_;
  const int tid = threadIdx.x, lane = tid & 63;
  const int w = __builtin_amdgcn_readfirstlane(tid >> 6);
  const int bh = blockIdx.x, b = bh >> 5;
  const unsigned g = lane >> 4, c = lane & 15;
  const unsigned lds0 = (unsigned)(size_t)smem;
  unsigned long long t0 = 0; unsigned pv = 0;
  if constexpr (C::STAMP || C::PACE) t0 = __builtin_amdgcn_s_memrealtime();

  const __amdgpu_buffer_rsrc_t srdK = __builtin_amdgcn_make_buffer_rsrc((void*)(kc + (size_t)bh * C_ * D_), 0, C_ * D_ * 2, 0x00020000);
  const __amdgpu_buffer_rsrc_t srdV = __builtin_amdgcn_make_buffer_rsrc((void*)(vc + (size_t)bh * C_ * D_), 0, C_ * D_ * 2, 0x00020000);

  unsigned vk[4], vv[2];
#pragma unroll
  for (int i = 0; i < 4; ++i) vk[i] = g * 256 + 16 * ((c ^ g) ^ (4 * i));
#pragma unroll
  for (int j = 0; j < 2; ++j) vv[j] = g * 256 + 16 * (c ^ (2 * g) ^ (8 * j));

  const unsigned ring = lds0 + w * WREG;
  auto step_off = [&](int s) -> unsigned { return C::ORD == 0 ? (unsigned)(s * NW + w) * SLOT : (unsigned)(w * NS + s) * SLOT; };
  auto issueK = [&](int s, int slot) {
    const unsigned o = step_off(s);
#pragma unroll
    for (int t = 0; t < TK; ++t) dma_tile<(C::NT >= 4 ? C::NT : (C::NT == 1 || C::NT == 2))>(ring + slot * SLOT + t * 4096, vk[0], vk[1], vk[2], vk[3], srdK, o + t * 4096);
  };
  auto issueV = [&](int s, int slot) {
    const unsigned o = step_off(s);
#pragma unroll
    for (int t = 0; t < TK; ++t) dma_tile<(C::NT >= 4 ? C::NT : (C::NT == 1 || C::NT == 3))>(ring + DEPTH * SLOT + slot * SLOT + t * 4096, vv[0], vv[1], vv[0], vv[1], srdV, o + t * 4096);
  };

  unsigned phase = 0;
  if constexpr (C::STG == 1) phase = (unsigned)(((w + 4 * (bh >> 3)) & 15) * (C::PACE / NS)) / 16u;
  if constexpr (C::STG == 2) phase = (unsigned)(w * (C::PACE / NS)) / (unsigned)NW;
  if constexpr (C::STG == 3) phase = (unsigned)((((bh * 4 + w) * 37) & 63) * (C::PACE / NS)) / 64u;
  if constexpr (C::XONLY >= 0) {
    unsigned xcc; asm volatile("s_getreg_b32 %0, hwreg(HW_REG_XCC_ID)" : "=s"(xcc));
    if ((int)(xcc & 7) != C::XONLY) phase = 0x40000000u;
  }
  constexpr int PPW = NW <= 16 ? 16 / NW : 1;
  auto pieces = [&]() {
    const size_t tb = (size_t)bh * T_ * D_ * 2;
#pragma unroll
    for (int j = 0; j < PPW; ++j) {
      const int pi = (w * PPW + j) & 15;
      const int kind = pi & 3, rg = pi >> 2;
      const unsigned row = 4 * rg + g;
      const char* src; unsigned dst;
      if (kind == 0)      { src = (const char*)q + tb + row * 256 + 16 * (c ^ row);              dst = C::LDS_Q + rg * 1024; }
      else if (kind == 1) { src = (const char*)kt + tb + row * 256 + 16 * (c ^ row);             dst = C::LDS_KT + rg * 1024; }
      else if (kind == 2) { src = (const char*)vt + tb + row * 256 + 16 * (c ^ (2 * (row & 7))); dst = C::LDS_VT + rg * 1024; }
      else                { src = (const char*)mask + (size_t)b * 512 + (lane & 31) * 16;        dst = C::LDS_MASK; }
      const unsigned m0v = __builtin_amdgcn_readfirstlane(lds0 + dst);
      asm volatile("s_nop 4\n\ts_mov_b32 m0, %0\n\ts_nop 0\n\tglobal_load_lds_dwordx4 %1, off" :: "s"(m0v), "v"(src) : "memory");
    }
    if constexpr (C::TOUCH) {
      const char* src = (const char*)out + tb + lane * 16;
      const unsigned m0v = lds0 + C::LDS_DUMMY;
      asm volatile("s_mov_b32 m0, %0\n\ts_nop 0\n\tglobal_load_lds_dwordx4 %1, off" :: "s"(m0v), "v"(src) : "memory");
    }
  };
  if constexpr (C::PRO == 0) {
    pieces();
#pragma unroll
    for (int s = 0; s < DEPTH; ++s) { issueK(s, s); issueV(s, s); }
  } else {
    issueK(0, 0); issueV(0, 0);
    pieces();
#pragma unroll
    for (int s = 1; s < DEPTH; ++s) { issueK(s, s); issueV(s, s); }
  }
  constexpr int PWAIT = C::PRO == 0 ? DEPTH * 2 * G : (DEPTH - 1) * 2 * G;

  unsigned ka[4], va[8];
#pragma unroll
  for (int kk = 0; kk < 4; ++kk) ka[kk] = c * 256 + 16 * ((g ^ c) ^ (4 * kk));
  {
    const unsigned qq = c >> 2, p = c & 3, row = 4 * g + qq;
#pragma unroll
    for (int dt = 0; dt < 8; ++dt) va[dt] = row * 256 + 32 * (dt ^ (row & 7)) + 8 * p;
  }

  bf16x8 qf[4];
  f32x4 acc[8];
#pragma unroll
  for (int dt = 0; dt < 8; ++dt) acc[dt] = f32x4{0.f, 0.f, 0.f, 0.f};
  float m = -INFINITY, l = 0.f;

  wait_vm<PWAIT>();
  if constexpr (C::STAMP) { unsigned t = (unsigned)__builtin_amdgcn_s_memrealtime(); asm volatile("s_waitcnt lgkmcnt(0)" ::: "memory"); pv = (lane == 17) ? t : pv; pv = (lane == 18) ? (unsigned)t0 : pv; }
  __builtin_amdgcn_s_barrier();
#pragma unroll
  for (int kk = 0; kk < 4; ++kk) qf[kk] = lds_b128(smem + C::LDS_Q + ka[kk]);

  if (w == NW - 1) {
    f32x4 s = {0.f, 0.f, 0.f, 0.f};
#pragma unroll
    for (int kk = 0; kk < 4; ++kk) s = __builtin_amdgcn_mfma_f32_16x16x32_bf16(lds_b128(smem + C::LDS_KT + ka[kk]), qf[kk], s, 0, 0, 0);
    const u32x2 mk = *(const u32x2 LAS*)(smem + C::LDS_MASK + c * 32 + g * 8);
    const float mv[4] = {__uint_as_float(mk[0] << 16), __uint_as_float(mk[0] & 0xffff0000u), __uint_as_float(mk[1] << 16), __uint_as_float(mk[1] & 0xffff0000u)};
    float t[4];
#pragma unroll
    for (int r = 0; r < 4; ++r) t[r] = (s[r] * kScale + (1.0f - mv[r]) * -1000000000.0f) * kL2E;
    const float mx = xg_max(fmaxf(fmaxf(t[0], t[1]), fmaxf(t[2], t[3])));
    m = mx;
    float p[4];
#pragma unroll
    for (int r = 0; r < 4; ++r) { p[r] = __builtin_amdgcn_exp2f(t[r] - mx); l += p[r]; }
    const u32x2 pw = {cvtpk(p[0], p[1]), cvtpk(p[2], p[3])};
    const s16x4 pf = __builtin_bit_cast(s16x4, pw);
#pragma unroll
    for (int dt = 0; dt < 8; ++dt) acc[dt] = __builtin_amdgcn_mfma_f32_16x16x16bf16_1k(lds_tr(smem + C::LDS_VT + va[dt]), pf, acc[dt], 0, 0, 0);
  }

  const char LAS* kring = smem + w * WREG;
  const char LAS* vring = kring + DEPTH * SLOT;

  auto step = [&](int s, auto slot_c, auto wk_c, auto wv_c, auto issue_c) {
    constexpr int slot = decltype(slot_c)::value, WK = decltype(wk_c)::value, WV = decltype(wv_c)::value;
    constexpr bool ISSUE = decltype(issue_c)::value;
    SB();
    wait_vm<WK>();
    if constexpr (C::STAMP) { if ((s % (NS / 16 > 0 ? NS / 16 : 1)) == 0) { unsigned t = (unsigned)__builtin_amdgcn_s_memrealtime(); asm volatile("s_waitcnt lgkmcnt(0)" ::: "memory"); pv = (lane == s / (NS / 16 > 0 ? NS / 16 : 1)) ? t : pv; } }
    unsigned tnow = 0;
    if constexpr (C::PACE && ISSUE) tnow = (unsigned)__builtin_amdgcn_s_memrealtime();
    bf16x8 kf[TK][4];
#pragma unroll
    for (int t = 0; t < TK; ++t)
#pragma unroll
      for (int kk = 0; kk < 4; ++kk) kf[t][kk] = lds_b128(kring + slot * SLOT + t * 4096 + ka[kk]);
    if constexpr (ISSUE) {
      wait_lgkm0();
      if constexpr (C::PACE) {
        const unsigned target = (unsigned)((s + 1) * C::PACE) / (unsigned)NS;
        while (tnow - (unsigned)t0 + phase < target) { __builtin_amdgcn_s_sleep(1); tnow = (unsigned)__builtin_amdgcn_s_memrealtime(); }
      }
      issueK(s + DEPTH, slot);
    }
    SB();
    f32x4 sc[TK];
#pragma unroll
    for (int t = 0; t < TK; ++t) {
      sc[t] = f32x4{0.f, 0.f, 0.f, 0.f};
#pragma unroll
      for (int kk = 0; kk < 4; ++kk) sc[t] = __builtin_amdgcn_mfma_f32_16x16x32_bf16(kf[t][kk], qf[kk], sc[t], 0, 0, 0);
    }
    float mx = fmaxf(fmaxf(sc[0][0], sc[0][1]), fmaxf(sc[0][2], sc[0][3]));
    if constexpr (TK == 2) mx = fmaxf(mx, fmaxf(fmaxf(sc[1][0], sc[1][1]), fmaxf(sc[1][2], sc[1][3])));
    mx = xg_max(mx) * kC;
    float mn;
    if constexpr (C::CRS) {
      if (__builtin_amdgcn_ballot_w64(mx > m) != 0) {
        mn = fmaxf(m, mx);
        const float alpha = __builtin_amdgcn_exp2f(m - mn);
        m = mn;
        l *= alpha;
#pragma unroll
        for (int dt = 0; dt < 8; ++dt) acc[dt] *= alpha;
      }
      mn = m;
    }
    float alpha = 1.f;
    if constexpr (!C::CRS) {
      mn = fmaxf(m, mx);
      alpha = __builtin_amdgcn_exp2f(m - mn);
      m = mn;
    }
    float p[4 * TK];
    float ps = 0.f;
#pragma unroll
    for (int t = 0; t < TK; ++t)
#pragma unroll
      for (int r = 0; r < 4; ++r) { p[4 * t + r] = __builtin_amdgcn_exp2f(fmaf(sc[t][r], kC, -mn)); ps += p[4 * t + r]; }
    if constexpr (C::CRS) l += ps; else l = fmaf(l, alpha, ps);
    if constexpr (!C::CRS) {
#pragma unroll
      for (int dt = 0; dt < 8; ++dt) acc[dt] *= alpha;
    }
    unsigned pw[2 * TK];
#pragma unroll
    for (int i = 0; i < 2 * TK; ++i) pw[i] = cvtpk(p[2 * i], p[2 * i + 1]);
    SB();
    wait_vm<WV>();
    s16x4 vf[8][TK];
#pragma unroll
    for (int dt = 0; dt < 8; ++dt)
#pragma unroll
      for (int t = 0; t < TK; ++t) vf[dt][t] = lds_tr(vring + slot * SLOT + t * 4096 + va[dt]);
    if constexpr (ISSUE) { wait_lgkm0(); issueV(s + DEPTH, slot); }
    SB();
    if constexpr (TK == 2) {
      const u32x4 pq = {pw[0], pw[1], pw[2], pw[3]};
      const bf16x8 pf = __builtin_bit_cast(bf16x8, pq);
#pragma unroll
      for (int dt = 0; dt < 8; ++dt) {
        const s16x8 a = {vf[dt][0][0], vf[dt][0][1], vf[dt][0][2], vf[dt][0][3], vf[dt][1][0], vf[dt][1][1], vf[dt][1][2], vf[dt][1][3]};
        acc[dt] = __builtin_amdgcn_mfma_f32_16x16x32_bf16(__builtin_bit_cast(bf16x8, a), pf, acc[dt], 0, 0, 0);
      }
    } else {
      const u32x2 pq = {pw[0], pw[1]};
      const s16x4 pf = __builtin_bit_cast(s16x4, pq);
#pragma unroll
      for (int dt = 0; dt < 8; ++dt) acc[dt] = __builtin_amdgcn_mfma_f32_16x16x16bf16_1k(vf[dt][0], pf, acc[dt], 0, 0, 0);
    }
  };
  using std::integral_constant;
  constexpr int WS = G * (2 * DEPTH - 1);
  constexpr int R = (NS - DEPTH) / DEPTH;
  for (int r = 0; r < R; ++r)
    sfor<DEPTH>([&](auto i) { step(r * DEPTH + i.value, i, integral_constant<int, WS>{}, integral_constant<int, WS>{}, integral_constant<bool, true>{}); });
  sfor<NS - R * DEPTH>([&](auto i) {
    constexpr int s = R * DEPTH + i.value;
    constexpr int last = (s + DEPTH - 1 < NS - 1) ? s + DEPTH - 1 : NS - 1;
    constexpr bool iss = s + DEPTH < NS;
    step(s, integral_constant<int, s % DEPTH>{}, integral_constant<int, G * (1 + 2 * (last - s))>{},
         integral_constant<int, G * (2 * (last - s) + (iss ? 1 : 0))>{}, integral_constant<bool, iss>{});
  });

  if constexpr (C::STAMP) { unsigned t = (unsigned)__builtin_amdgcn_s_memrealtime(); asm volatile("s_waitcnt lgkmcnt(0)" ::: "memory"); pv = (lane == 16) ? t : pv; }
  l = xg_sum(l);
  {
    char LAS* mr = smem + w * WREG;
#pragma unroll
    for (int dt = 0; dt < 8; ++dt) *(f32x4 LAS*)(mr + c * MROW + (16 * dt + 4 * g) * 4) = acc[dt];
    if (g == 0) { *(float LAS*)(mr + 16 * MROW + c * 8) = m; *(float LAS*)(mr + 16 * MROW + c * 8 + 4) = l; }
  }
  __syncthreads();
  if (tid < 256) {
    const int qi = tid >> 4, dc = tid & 15;
    float mw[NW], lw[NW], M = -INFINITY;
#pragma unroll
    for (int x = 0; x < NW; ++x) {
      mw[x] = *(const float LAS*)(smem + x * WREG + 16 * MROW + qi * 8);
      lw[x] = *(const float LAS*)(smem + x * WREG + 16 * MROW + qi * 8 + 4);
      M = fmaxf(M, mw[x]);
    }
    float L = 0.f;
    f32x4 o0 = {0.f, 0.f, 0.f, 0.f}, o1 = {0.f, 0.f, 0.f, 0.f};
#pragma unroll
    for (int x = 0; x < NW; ++x) {
      const float f = __builtin_amdgcn_exp2f(mw[x] - M);
      L = fmaf(f, lw[x], L);
      const f32x4 a0 = *(const f32x4 LAS*)(smem + x * WREG + qi * MROW + dc * 32);
      const f32x4 a1 = *(const f32x4 LAS*)(smem + x * WREG + qi * MROW + dc * 32 + 16);
      o0 += f * a0; o1 += f * a1;
    }
    const float inv = 1.0f / L;
    const u32x4 ow = {cvtpk(o0[0] * inv, o0[1] * inv), cvtpk(o0[2] * inv, o0[3] * inv), cvtpk(o1[0] * inv, o1[1] * inv), cvtpk(o1[2] * inv, o1[3] * inv)};
    u32x4* op = (u32x4*)(out + ((size_t)bh * T_ + qi) * D_ + dc * 8);
    if constexpr (C::OST == 1) __builtin_nontemporal_store(ow, op);
    else if constexpr (C::OST == 2) asm volatile("global_store_dwordx4 %0, %1, off sc0 sc1\n\ts_nop 1" :: "v"(op), "v"(ow) : "memory");
    else *op = ow;
  }
  if constexpr (C::STAMP) {
    unsigned long long t1 = __builtin_amdgcn_s_memrealtime();
    unsigned xcc; asm volatile("s_getreg_b32 %0, hwreg(HW_REG_XCC_ID)" : "=s"(xcc));
    if (tid == 0) { stamps[2 * bh] = t0; stamps[2 * bh + 1] = t1; stamps[8192 + bh] = xcc & 0xf; }
    if (w == 0 && lane < 32) stamps[16384 + bh * 32 + lane] = pv;
  }
}

}

#define NTHR(C) (C::NW * 64)

extern "C" void kernel_launch(void* const* d_in, const int* in_sizes, int n_in, void* d_out, int out_size, void* d_ws, size_t ws_size,
                              hipStream_t stream) {
  (void)in_sizes; (void)n_in; (void)out_size; (void)ws_size;
  using C = ta::Cfg<4, 1, 2, 0, 1, 0, 0, 7450, 0, 0, 0>;
  (void)hipFuncSetAttribute((const void*)ta::tree_attn<C>, hipFuncAttributeMaxDynamicSharedMemorySize, C::LDS_TOTAL);
  hipLaunchKernelGGL((ta::tree_attn<C>), dim3(256), dim3(NTHR(C)), C::LDS_TOTAL, stream,
                     (const uint16_t*)d_in[0], (const uint16_t*)d_in[1], (const uint16_t*)d_in[2], (const uint16_t*)d_in[3],
                     (const uint16_t*)d_in[4], (const uint16_t*)d_in[5], (uint16_t*)d_out, (unsigned long long*)d_ws);
}
```

```cpp
#include <hip/hip_runtime.h>
#include <hip/hip_bf16.h>
#include <stdint.h>
#include <utility>

#define TA_PACE 7480
#define TA_DEPTH 2

namespace {

constexpr int kBH = 8 * 32, kT = 16, kC = 4096, kD = 128;
constexpr int NW = 4;
constexpr int DEPTH = TA_DEPTH;
constexpr int NS = kC / 16 / NW;
constexpr int TILE = 4096;
constexpr int G = 4;
constexpr int WREG = DEPTH * 2 * TILE;
constexpr int MROW = 528;
constexpr int LDS_Q = NW * WREG, LDS_KT = LDS_Q + TILE, LDS_VT = LDS_KT + TILE, LDS_MASK = LDS_VT + TILE;
constexpr int LDS_USED = LDS_MASK + 1024;
constexpr int LDS_TOTAL = LDS_USED > 83968 ? LDS_USED : 83968;
static_assert(16 * MROW + 128 <= WREG && LDS_TOTAL <= 163840 && DEPTH * 2 * G + 4 <= 63, "layout");

typedef __attribute__((ext_vector_type(8))) __bf16 bf16x8;
typedef __attribute__((ext_vector_type(4))) short s16x4;
typedef __attribute__((ext_vector_type(4))) float f32x4;
typedef __attribute__((ext_vector_type(2))) float f32x2;
typedef __attribute__((ext_vector_type(4))) unsigned u32x4;
typedef __attribute__((ext_vector_type(2))) unsigned u32x2;
#define LAS __attribute__((address_space(3)))
#define SB() __builtin_amdgcn_sched_barrier(0)

constexpr float kScale = 0.088388347648318440f;
constexpr float kL2E = 1.4426950408889634f;
constexpr float kSL = kScale * kL2E;

__device__ __forceinline__ unsigned cvtpk(float lo, float hi) {
  typedef __attribute__((ext_vector_type(2))) __bf16 bf16x2;
  const f32x2 v = {lo, hi};
  return __builtin_bit_cast(unsigned, __builtin_convertvector(v, bf16x2));
}
__device__ __forceinline__ float xg_max(float x) {
  auto s = __builtin_amdgcn_permlane16_swap(__float_as_uint(x), __float_as_uint(x), false, false);
  x = fmaxf(__uint_as_float(s[0]), __uint_as_float(s[1]));
  auto t = __builtin_amdgcn_permlane32_swap(__float_as_uint(x), __float_as_uint(x), false, false);
  return fmaxf(__uint_as_float(t[0]), __uint_as_float(t[1]));
}
__device__ __forceinline__ float xg_sum(float x) {
  auto s = __builtin_amdgcn_permlane16_swap(__float_as_uint(x), __float_as_uint(x), false, false);
  x = __uint_as_float(s[0]) + __uint_as_float(s[1]);
  auto t = __builtin_amdgcn_permlane32_swap(__float_as_uint(x), __float_as_uint(x), false, false);
  return __uint_as_float(t[0]) + __uint_as_float(t[1]);
}

__device__ __forceinline__ void dma_tile(unsigned m0v, unsigned v0, unsigned v1, unsigned v2, unsigned v3,
                                         __amdgpu_buffer_rsrc_t srd, unsigned soff) {
  asm volatile(
      "s_mov_b32 m0, %0\n\t"
      "s_nop 0\n\t"
      "buffer_load_dwordx4 %1, %5, %6 offen nt lds\n\t"
      "buffer_load_dwordx4 %2, %5, %6 offen offset:1024 nt lds\n\t"
      "buffer_load_dwordx4 %3, %5, %6 offen offset:2048 nt lds\n\t"
      "buffer_load_dwordx4 %4, %5, %6 offen offset:3072 nt lds"
      :: "s"(m0v), "v"(v0), "v"(v1), "v"(v2), "v"(v3), "s"(srd), "s"(soff) : "memory");
}
__device__ __forceinline__ void dma_piece(unsigned m0v, const void* src) {
  asm volatile("s_nop 4\n\ts_mov_b32 m0, %0\n\ts_nop 0\n\tglobal_load_lds_dwordx4 %1, off" :: "s"(m0v), "v"(src) : "memory");
}
template <int N> __device__ __forceinline__ void wait_vm() {
  static_assert(N >= 0 && N <= 63, "vmcnt");
  asm volatile("s_waitcnt vmcnt(%0)" :: "i"(N) : "memory");
}
__device__ __forceinline__ void wait_lgkm0() { asm volatile("s_waitcnt lgkmcnt(0)" ::: "memory"); }
__device__ __forceinline__ unsigned clock10ns() { return (unsigned)__builtin_amdgcn_s_memrealtime(); }
__device__ __forceinline__ bf16x8 lds_b128(const char LAS* p) { return *(const bf16x8 LAS*)p; }
__device__ __forceinline__ s16x4 lds_tr(const char LAS* p) { return __builtin_amdgcn_ds_read_tr16_b64_v4i16((s16x4 LAS*)p); }

template <class F, int... I> __device__ __forceinline__ void sfor_impl(F&& f, std::integer_sequence<int, I...>) { (f(std::integral_constant<int, I>{}), ...); }
template <int N, class F> __device__ __forceinline__ void sfor(F&& f) { sfor_impl(f, std::make_integer_sequence<int, N>{}); }

}

__global__ __launch_bounds__(NW * 64, 2) void tree_attn_kernel(
    const uint16_t* __restrict__ q, const uint16_t* __restrict__ kc, const uint16_t* __restrict__ vc,
    const uint16_t* __restrict__ kt, const uint16_t* __restrict__ vt, const uint16_t* __restrict__ mask,
    uint16_t* __restrict__ out) {
  extern __shared__ __attribute__((aligned(16))) char smem_[];
  char LAS* smem = (char LAS*)smem_;
  unsigned long long tstart;
  asm volatile("s_memrealtime %0" : "=s"(tstart));
  const int tid = threadIdx.x, lane = tid & 63;
  const int w = __builtin_amdgcn_readfirstlane(tid >> 6);
  const int bh = blockIdx.x;
  const unsigned g = lane >> 4, c = lane & 15;
  const unsigned lds0 = (unsigned)(size_t)smem;

  const __amdgpu_buffer_rsrc_t srdK = __builtin_amdgcn_make_buffer_rsrc((void*)(kc + (size_t)bh * kC * kD), 0, kC * kD * 2, 0x00020000);
  const __amdgpu_buffer_rsrc_t srdV = __builtin_amdgcn_make_buffer_rsrc((void*)(vc + (size_t)bh * kC * kD), 0, kC * kD * 2, 0x00020000);

  unsigned vk[4], vv[2];
#pragma unroll
  for (int i = 0; i < 4; ++i) vk[i] = g * 256 + 16 * ((c ^ g) ^ (4 * i));
#pragma unroll
  for (int j = 0; j < 2; ++j) vv[j] = g * 256 + 16 * (c ^ (2 * g) ^ (8 * j));

  const unsigned ring = lds0 + w * WREG;
  auto issueK = [&](int s, int slot) { dma_tile(ring + slot * TILE, vk[0], vk[1], vk[2], vk[3], srdK, (unsigned)(s * NW + w) * TILE); };
  auto issueV = [&](int s, int slot) { dma_tile(ring + (DEPTH + slot) * TILE, vv[0], vv[1], vv[0], vv[1], srdV, (unsigned)(s * NW + w) * TILE); };

  issueK(0, 0);
  {
    const size_t tb = (size_t)bh * kT * kD * 2;
    const unsigned row = 4 * w + g;
    dma_piece(lds0 + LDS_Q + w * 1024, (const char*)q + tb + row * 256 + 16 * (c ^ row));
    dma_piece(lds0 + LDS_KT + w * 1024, (const char*)kt + tb + row * 256 + 16 * (c ^ row));
    dma_piece(lds0 + LDS_VT + w * 1024, (const char*)vt + tb + row * 256 + 16 * (c ^ (2 * (row & 7))));
    dma_piece(lds0 + LDS_MASK, (const char*)mask + (size_t)(bh >> 5) * 512 + (lane & 31) * 16);
  }
  issueV(0, 0);
#pragma unroll
  for (int s = 1; s < DEPTH; ++s) { issueK(s, s); issueV(s, s); }
  asm volatile("s_waitcnt lgkmcnt(0)" : "+s"(tstart));
  const unsigned t0 = (unsigned)tstart;

  unsigned ka[4], va[8];
#pragma unroll
  for (int kk = 0; kk < 4; ++kk) ka[kk] = c * 256 + 16 * ((g ^ c) ^ (4 * kk));
  {
    const unsigned row = 4 * g + (c >> 2), p = c & 3;
#pragma unroll
    for (int dt = 0; dt < 8; ++dt) va[dt] = row * 256 + 32 * (dt ^ (row & 7)) + 8 * p;
  }

  bf16x8 qf[4];
  f32x4 acc[8];
#pragma unroll
  for (int dt = 0; dt < 8; ++dt) acc[dt] = f32x4{0.f, 0.f, 0.f, 0.f};
  float m = -INFINITY, l = 0.f;

  wait_vm<G * (2 * DEPTH - 1)>();
  __builtin_amdgcn_s_barrier();
#pragma unroll
  for (int kk = 0; kk < 4; ++kk) qf[kk] = lds_b128(smem + LDS_Q + ka[kk]);

  if (w == NW - 1) {
    f32x4 s = {0.f, 0.f, 0.f, 0.f};
#pragma unroll
    for (int kk = 0; kk < 4; ++kk) s = __builtin_amdgcn_mfma_f32_16x16x32_bf16(lds_b128(smem + LDS_KT + ka[kk]), qf[kk], s, 0, 0, 0);
    const u32x2 mk = *(const u32x2 LAS*)(smem + LDS_MASK + c * 32 + g * 8);
    const float mv[4] = {__uint_as_float(mk[0] << 16), __uint_as_float(mk[0] & 0xffff0000u),
                         __uint_as_float(mk[1] << 16), __uint_as_float(mk[1] & 0xffff0000u)};
    float t[4];
#pragma unroll
    for (int r = 0; r < 4; ++r) t[r] = (s[r] * kScale + (1.0f - mv[r]) * -1000000000.0f) * kL2E;
    m = xg_max(fmaxf(fmaxf(t[0], t[1]), fmaxf(t[2], t[3])));
    float p[4];
#pragma unroll
    for (int r = 0; r < 4; ++r) { p[r] = __builtin_amdgcn_exp2f(t[r] - m); l += p[r]; }
    const u32x2 pw = {cvtpk(p[0], p[1]), cvtpk(p[2], p[3])};
#pragma unroll
    for (int dt = 0; dt < 8; ++dt)
      acc[dt] = __builtin_amdgcn_mfma_f32_16x16x16bf16_1k(lds_tr(smem + LDS_VT + va[dt]), __builtin_bit_cast(s16x4, pw), acc[dt], 0, 0, 0);
  }

  const char LAS* kring = smem + w * WREG;
  const char LAS* vring = kring + DEPTH * TILE;

  auto step = [&](int s, auto slot_c, auto wk_c, auto wv_c, auto issue_c) {
    constexpr int slot = decltype(slot_c)::value, WK = decltype(wk_c)::value, WV = decltype(wv_c)::value;
    constexpr bool ISSUE = decltype(issue_c)::value;
    SB();
    wait_vm<WK>();
    unsigned tnow = 0;
    if constexpr (ISSUE) tnow = clock10ns();
    bf16x8 kf[4];
#pragma unroll
    for (int kk = 0; kk < 4; ++kk) kf[kk] = lds_b128(kring + slot * TILE + ka[kk]);
    if constexpr (ISSUE) {
      wait_lgkm0();
      const unsigned target = (unsigned)((s + 1) * TA_PACE) / (unsigned)NS;
      while (tnow - t0 < target) { __builtin_amdgcn_s_sleep(1); tnow = clock10ns(); }
      issueK(s + DEPTH, slot);
    }
    SB();
    f32x4 sc = {0.f, 0.f, 0.f, 0.f};
#pragma unroll
    for (int kk = 0; kk < 4; ++kk) sc = __builtin_amdgcn_mfma_f32_16x16x32_bf16(kf[kk], qf[kk], sc, 0, 0, 0);
    const float mx = xg_max(fmaxf(fmaxf(sc[0], sc[1]), fmaxf(sc[2], sc[3]))) * kSL;
    const float mn = fmaxf(m, mx);
    const float alpha = __builtin_amdgcn_exp2f(m - mn);
    m = mn;
    float p[4];
#pragma unroll
    for (int r = 0; r < 4; ++r) p[r] = __builtin_amdgcn_exp2f(fmaf(sc[r], kSL, -mn));
    l = fmaf(l, alpha, (p[0] + p[1]) + (p[2] + p[3]));
#pragma unroll
    for (int dt = 0; dt < 8; ++dt) acc[dt] *= alpha;
    const u32x2 pw = {cvtpk(p[0], p[1]), cvtpk(p[2], p[3])};
    SB();
    wait_vm<WV>();
    s16x4 vf[8];
#pragma unroll
    for (int dt = 0; dt < 8; ++dt) vf[dt] = lds_tr(vring + slot * TILE + va[dt]);
    if constexpr (ISSUE) { wait_lgkm0(); issueV(s + DEPTH, slot); }
    SB();
#pragma unroll
    for (int dt = 0; dt < 8; ++dt) acc[dt] = __builtin_amdgcn_mfma_f32_16x16x16bf16_1k(vf[dt], __builtin_bit_cast(s16x4, pw), acc[dt], 0, 0, 0);
  };
  using std::integral_constant;
  constexpr int WS = G * (2 * DEPTH - 1);
  constexpr int R = (NS - DEPTH) / DEPTH;
  for (int r = 0; r < R; ++r)
    sfor<DEPTH>([&](auto i) { step(r * DEPTH + i.value, i, integral_constant<int, WS>{}, integral_constant<int, WS>{}, integral_constant<bool, true>{}); });
  sfor<NS - R * DEPTH>([&](auto i) {
    constexpr int s = R * DEPTH + i.value;
    constexpr int last = (s + DEPTH - 1 < NS - 1) ? s + DEPTH - 1 : NS - 1;
    constexpr bool iss = s + DEPTH < NS;
    step(s, integral_constant<int, s % DEPTH>{}, integral_constant<int, G * (1 + 2 * (last - s))>{},
         integral_constant<int, G * (2 * (last - s) + (iss ? 1 : 0))>{}, integral_constant<bool, iss>{});
  });

  l = xg_sum(l);
  {
    char LAS* mr = smem + w * WREG;
#pragma unroll
    for (int dt = 0; dt < 8; ++dt) *(f32x4 LAS*)(mr + c * MROW + (16 * dt + 4 * g) * 4) = acc[dt];
    if (g == 0) *(f32x2 LAS*)(mr + 16 * MROW + c * 8) = f32x2{m, l};
  }
  __syncthreads();
  {
    const int qi = tid >> 4, dc = tid & 15;
    float mw[NW], lw[NW], M = -INFINITY;
#pragma unroll
    for (int x = 0; x < NW; ++x) {
      mw[x] = *(const float LAS*)(smem + x * WREG + 16 * MROW + qi * 8);
      lw[x] = *(const float LAS*)(smem + x * WREG + 16 * MROW + qi * 8 + 4);
      M = fmaxf(M, mw[x]);
    }
    float L = 0.f;
    f32x4 o0 = {0.f, 0.f, 0.f, 0.f}, o1 = {0.f, 0.f, 0.f, 0.f};
#pragma unroll
    for (int x = 0; x < NW; ++x) {
      const float f = __builtin_amdgcn_exp2f(mw[x] - M);
      L = fmaf(f, lw[x], L);
      o0 += f * *(const f32x4 LAS*)(smem + x * WREG + qi * MROW + dc * 32);
      o1 += f * *(const f32x4 LAS*)(smem + x * WREG + qi * MROW + dc * 32 + 16);
    }
    const float inv = 1.0f / L;
    const u32x4 ow = {cvtpk(o0[0] * inv, o0[1] * inv), cvtpk(o0[2] * inv, o0[3] * inv),
                      cvtpk(o1[0] * inv, o1[1] * inv), cvtpk(o1[2] * inv, o1[3] * inv)};
    *(u32x4*)(out + ((size_t)bh * kT + qi) * kD + dc * 8) = ow;
  }
}

extern "C" void kernel_launch(void* const* d_in, const int* in_sizes, int n_in, void* d_out, int out_size, void* d_ws, size_t ws_size,
                              hipStream_t stream) {
  (void)in_sizes; (void)n_in; (void)out_size; (void)d_ws; (void)ws_size;
  (void)hipFuncSetAttribute((const void*)tree_attn_kernel, hipFuncAttributeMaxDynamicSharedMemorySize, LDS_TOTAL);
  hipLaunchKernelGGL(tree_attn_kernel, dim3(kBH), dim3(NW * 64), LDS_TOTAL, stream,
                     (const uint16_t*)d_in[0], (const uint16_t*)d_in[1], (const uint16_t*)d_in[2], (const uint16_t*)d_in[3],
                     (const uint16_t*)d_in[4], (const uint16_t*)d_in[5], (uint16_t*)d_out);
}
```

```cpp
#include <hip/hip_runtime.h>
#include <hip/hip_bf16.h>
#include <stdint.h>
#include <utility>

namespace ta {

constexpr int B_ = 8, H_ = 32, T_ = 16, C_ = 4096, D_ = 128;
constexpr int MROW = 528;

typedef __attribute__((ext_vector_type(8))) __bf16 bf16x8;
typedef __attribute__((ext_vector_type(4))) short s16x4;
typedef __attribute__((ext_vector_type(8))) short s16x8;
typedef __attribute__((ext_vector_type(4))) float f32x4;
typedef __attribute__((ext_vector_type(4))) unsigned u32x4;
typedef __attribute__((ext_vector_type(2))) unsigned u32x2;
#define LAS __attribute__((address_space(3)))
#define SB() __builtin_amdgcn_sched_barrier(0)

__device__ __forceinline__ unsigned cvtpk(float lo, float hi) {
  typedef __attribute__((ext_vector_type(2))) __bf16 bf16x2;
  typedef __attribute__((ext_vector_type(2))) float f32x2;
  f32x2 v = {lo, hi};
  bf16x2 r = __builtin_convertvector(v, bf16x2);
  return __builtin_bit_cast(unsigned, r);
}
__device__ __forceinline__ float xg_max(float x) {
  auto s = __builtin_amdgcn_permlane16_swap(__float_as_uint(x), __float_as_uint(x), false, false);
  x = fmaxf(__uint_as_float(s[0]), __uint_as_float(s[1]));
  auto t = __builtin_amdgcn_permlane32_swap(__float_as_uint(x), __float_as_uint(x), false, false);
  return fmaxf(__uint_as_float(t[0]), __uint_as_float(t[1]));
}
__device__ __forceinline__ float xg_sum(float x) {
  auto s = __builtin_amdgcn_permlane16_swap(__float_as_uint(x), __float_as_uint(x), false, false);
  x = __uint_as_float(s[0]) + __uint_as_float(s[1]);
  auto t = __builtin_amdgcn_permlane32_swap(__float_as_uint(x), __float_as_uint(x), false, false);
  return __uint_as_float(t[0]) + __uint_as_float(t[1]);
}

#define DMA4(POL) asm volatile( \
      "s_mov_b32 m0, %0\n\t" \
      "s_nop 0\n\t" \
      "buffer_load_dwordx4 %1, %5, %6 offen" POL " lds\n\t" \
      "buffer_load_dwordx4 %2, %5, %6 offen offset:1024" POL " lds\n\t" \
      "buffer_load_dwordx4 %3, %5, %6 offen offset:2048" POL " lds\n\t" \
      "buffer_load_dwordx4 %4, %5, %6 offen offset:3072" POL " lds" \
      :: "s"(m0v), "v"(v0), "v"(v1), "v"(v2), "v"(v3), "s"(srd), "s"(soff) : "memory")
template <int NT>
__device__ __forceinline__ void dma_tile(unsigned m0v, unsigned v0, unsigned v1, unsigned v2, unsigned v3,
                                         __amdgpu_buffer_rsrc_t srd, unsigned soff) {
  if constexpr (NT == 1) DMA4(" nt");
  else if constexpr (NT == 4) DMA4(" nt sc1");
  else if constexpr (NT == 5) DMA4(" sc0 nt sc1");
  else if constexpr (NT == 6) DMA4(" sc1");
  else DMA4("");
}
template <int N> __device__ __forceinline__ void wait_vm() { static_assert(N >= 0 && N <= 63, "vmcnt"); asm volatile("s_waitcnt vmcnt(%0)" :: "i"(N) : "memory"); }
__device__ __forceinline__ void wait_lgkm0() { asm volatile("s_waitcnt lgkmcnt(0)" ::: "memory"); }

constexpr float kScale = 0.088388347648318440f;
constexpr float kL2E = 1.4426950408889634f;
constexpr float kC = kScale * kL2E;

__device__ __forceinline__ bf16x8 lds_b128(const char LAS* p) { return *(const bf16x8 LAS*)p; }
__device__ __forceinline__ s16x4 lds_tr(const char LAS* p) { return __builtin_amdgcn_ds_read_tr16_b64_v4i16((s16x4 LAS*)p); }

template <class F, int... I> __device__ __forceinline__ void sfor_impl(F&& f, std::integer_sequence<int, I...>) { (f(std::integral_constant<int, I>{}), ...); }
template <int N, class F> __device__ __forceinline__ void sfor(F&& f) { sfor_impl(f, std::make_integer_sequence<int, N>{}); }

template <int NW_, int TK_, int DEPTH_, int ORD_ = 0, int NT_ = 1, int STAMP_ = 0, int CRS_ = 0, int PACE_ = 0, int PRO_ = 0, int TOUCH_ = 0, int OST_ = 0, int STG_ = 0, int XONLY_ = -1, int PF_ = 0, int PFG_ = 64, int PFL_ = 0, int FOL_ = 0, int SLACK_ = 40, int FDBG_ = 0>
struct Cfg {
  static constexpr int NW = NW_, TK = TK_, DEPTH = DEPTH_, ORD = ORD_, NT = NT_, STAMP = STAMP_, CRS = CRS_, PACE = PACE_, PRO = PRO_, TOUCH = TOUCH_, OST = OST_, STG = STG_, XONLY = XONLY_, PF = PF_, PFG = PFG_, PFL = PFL_, FOL = FOL_, SLACK = SLACK_, FDBG = FDBG_;
  static constexpr int NTHREADS = (NW + ((PF_ == 1 || FOL_) ? 1 : 0)) * 64;
  static constexpr int SLOT = TK * 4096;
  static constexpr int RING = DEPTH * 2 * SLOT;
  static constexpr int NS = C_ / (16 * TK) / NW;
  static constexpr int MRG = (16 * MROW + 128 + 255) / 256 * 256;
  static constexpr int WREG = RING > MRG ? RING : MRG;
  static constexpr int LDS_Q = NW * WREG, LDS_KT = LDS_Q + 4096, LDS_VT = LDS_KT + 4096, LDS_MASK = LDS_VT + 4096;
  static constexpr int LDS_DUMMY = LDS_MASK + 1024;
  static constexpr int LDS_CTL = LDS_DUMMY + 1024;
  static constexpr int LDS_USED = LDS_CTL + 64;
  static constexpr int LDS_TOTAL = LDS_USED > 83968 ? LDS_USED : 83968;
  static_assert(LDS_TOTAL <= 163840, "LDS");
  static_assert(DEPTH * 2 * TK * 4 <= 63 - 16 / NW + 0 || DEPTH * 2 * TK * 4 <= 63, "vmcnt range");
};

template <class C>
__global__ __launch_bounds__(C::NTHREADS, C::NW >= 16 ? 4 : 2) void tree_attn(
    const uint16_t* __restrict__ q, const uint16_t* __restrict__ kc, const uint16_t* __restrict__ vc,
    const uint16_t* __restrict__ kt, const uint16_t* __restrict__ vt, const uint16_t* __restrict__ mask,
    uint16_t* __restrict__ out, unsigned long long* __restrict__ stamps) {
  constexpr int NW = C::NW, TK = C::TK, DEPTH = C::DEPTH, NS = C::NS, SLOT = C::SLOT, WREG = C::WREG, G = 4 * TK;
  extern __shared__ __attribute__((aligned(16))) char smem_[];
  char LAS* smem = (char LAS*)smem_;
  const int tid = threadIdx.x, lane = tid & 63;
  const int w = __builtin_amdgcn_readfirstlane(tid >> 6);
  const int bh = blockIdx.x, b = bh >> 5;
  const unsigned g = lane >> 4, c = lane & 15;
  const unsigned lds0 = (unsigned)(size_t)smem;
  unsigned long long t0 = 0; unsigned pv = 0;
  if constexpr (C::STAMP || C::PACE || C::PF || C::FOL) t0 = __builtin_amdgcn_s_memrealtime();

  const __amdgpu_buffer_rsrc_t srdK = __builtin_amdgcn_make_buffer_rsrc((void*)(kc + (size_t)bh * C_ * D_), 0, C_ * D_ * 2, 0x00020000);
  const __amdgpu_buffer_rsrc_t srdV = __builtin_amdgcn_make_buffer_rsrc((void*)(vc + (size_t)bh * C_ * D_), 0, C_ * D_ * 2, 0x00020000);

  if constexpr (C::FOL) {
    if (w == NW) {
      constexpr unsigned P = C::FOL;
      typedef __attribute__((address_space(1))) unsigned long long gu64;
      gu64* line = (gu64*)stamps + (C::STAMP ? 65536 : 0) + (bh >> 3) * 32;
      volatile unsigned LAS* ctl = (volatile unsigned LAS*)(smem + C::LDS_CTL);
      const unsigned T0 = (unsigned)t0;
      if (lane < 8) ctl[lane] = lane >= 4 ? T0 : 0u;
      asm volatile("s_waitcnt lgkmcnt(0)" ::: "memory");
      __builtin_amdgcn_s_barrier();
      unsigned tb = ((T0 + 300u) | (P - 1)) + 1u;
      int kper = 0; (void)kper;
      while (true) {
        unsigned done = ctl[1];
        while ((int)((unsigned)__builtin_amdgcn_s_memrealtime() - tb) < 0 && !done) { __builtin_amdgcn_s_sleep(8); done = ctl[1]; }
        if (__builtin_amdgcn_readfirstlane(done)) break;
        int e = (int)(ctl[4 + (lane & 3)] - T0);
        e = max(e, __shfl_xor(e, 1)); e = max(e, __shfl_xor(e, 2));
        if (C::FDBG != 1 && lane == 0) __hip_atomic_store(line + (bh & 7), ((unsigned long long)tb << 32) | (unsigned)(T0 + e), __ATOMIC_RELAXED, __HIP_MEMORY_SCOPE_AGENT);
        done = 0;
        while ((int)((unsigned)__builtin_amdgcn_s_memrealtime() - (tb + P / 2)) < 0 && !done) { __builtin_amdgcn_s_sleep(8); done = ctl[1]; }
        if (__builtin_amdgcn_readfirstlane(done)) break;
        int d = 0;
        if (C::FDBG != 1) {
          const unsigned long long v = __hip_atomic_load(line + (lane & 7), __ATOMIC_RELAXED, __HIP_MEMORY_SCOPE_AGENT);
          const unsigned tag = (unsigned)(v >> 32);
          const bool valid = (tag - (T0 - 300u)) <= (tb - (T0 - 300u));
          d = valid ? (int)((unsigned)v - T0) : 0;
          d = max(d, __shfl_xor(d, 1)); d = max(d, __shfl_xor(d, 2)); d = max(d, __shfl_xor(d, 4));
        }
        int delta = min(max(d - C::SLACK, 0), 1500);
        if (C::FDBG == 2) delta = 0;
        if (lane == 0) ctl[0] = (unsigned)delta;
        if constexpr (C::STAMP) { if (lane == 0 && kper < 20) { unsigned* dbg = (unsigned*)stamps + 160000 + (bh * 20 + kper) * 4; dbg[0] = e; dbg[1] = d; dbg[2] = delta; dbg[3] = tb - T0; } kper++; }
        tb += P;
      }
      return;
    }
  }
  if constexpr (C::PF == 1) {
    if (w == NW) {
      constexpr int NF = 1024 / C::PFG;
      const unsigned dummy = lds0 + C::LDS_DUMMY;
      const unsigned zero = 0;
      bool barrier_done = false;
      for (int f = 1; f < NF; ++f) {
        const int due = f * (C::PACE ? C::PACE : 7500) / NF - C::PFL;
        if (due > 0 && !barrier_done) { __builtin_amdgcn_s_barrier(); barrier_done = true; }
        if (due > 0) while ((int)((unsigned)__builtin_amdgcn_s_memrealtime() - (unsigned)t0) < due) __builtin_amdgcn_s_sleep(8);
        asm volatile("s_mov_b32 m0, %0\n\ts_nop 0\n\tbuffer_load_dword %1, %2, %4 offen nt lds\n\tbuffer_load_dword %1, %3, %4 offen nt lds"
                     :: "s"(dummy), "v"(zero), "s"(srdK), "s"(srdV), "s"((unsigned)f * C::PFG * 1024u) : "memory");
      }
      if (!barrier_done) __builtin_amdgcn_s_barrier();
      asm volatile("s_waitcnt vmcnt(0)" ::: "memory");
      return;
    }
  }
  unsigned vk[4], vv[2];
#pragma unroll
  for (int i = 0; i < 4; ++i) vk[i] = g * 256 + 16 * ((c ^ g) ^ (4 * i));
#pragma unroll
  for (int j = 0; j < 2; ++j) vv[j] = g * 256 + 16 * (c ^ (2 * g) ^ (8 * j));

  const unsigned ring = lds0 + w * WREG;
  auto step_off = [&](int s) -> unsigned {
    if constexpr (C::ORD == 0) return (unsigned)(s * NW + w) * SLOT;
    else if constexpr (C::ORD == 1) return (unsigned)(w * NS + s) * SLOT;
    else if constexpr (C::ORD >= 100) return (unsigned)(((s * NW + w) + (C::ORD - 100) * bh) & (NS * NW - 1)) * SLOT;
    else return (unsigned)(((s * NW + w) ^ (((bh * (C::ORD - 10)) & (NS - 1)) * NW)) & (NS * NW - 1)) * SLOT;
  };
  auto issueK = [&](int s, int slot) {
    const unsigned o = step_off(s);
#pragma unroll
    for (int t = 0; t < TK; ++t) dma_tile<(C::NT >= 4 ? C::NT : (C::NT == 1 || C::NT == 2))>(ring + slot * SLOT + t * 4096, vk[0], vk[1], vk[2], vk[3], srdK, o + t * 4096);
  };
  auto issueV = [&](int s, int slot) {
    const unsigned o = step_off(s);
#pragma unroll
    for (int t = 0; t < TK; ++t) dma_tile<(C::NT >= 4 ? C::NT : (C::NT == 1 || C::NT == 3))>(ring + DEPTH * SLOT + slot * SLOT + t * 4096, vv[0], vv[1], vv[0], vv[1], srdV, o + t * 4096);
  };

  unsigned phase = 0;
  if constexpr (C::STG == 1) phase = (unsigned)(((w + 4 * (bh >> 3)) & 15) * (C::PACE / NS)) / 16u;
  if constexpr (C::STG == 2) phase = (unsigned)(w * (C::PACE / NS)) / (unsigned)NW;
  if constexpr (C::STG == 3) phase = (unsigned)((((bh * 4 + w) * 37) & 63) * (C::PACE / NS)) / 64u;
  if constexpr (C::XONLY >= 0) {
    unsigned xcc; asm volatile("s_getreg_b32 %0, hwreg(HW_REG_XCC_ID)" : "=s"(xcc));
    if ((int)(xcc & 7) != C::XONLY) phase = 0x40000000u;
  }
  constexpr int PPW = NW <= 16 ? 16 / NW : 1;
  auto pieces = [&]() {
    const size_t tb = (size_t)bh * T_ * D_ * 2;
#pragma unroll
    for (int j = 0; j < PPW; ++j) {
      const int pi = (w * PPW + j) & 15;
      const int kind = pi & 3, rg = pi >> 2;
      const unsigned row = 4 * rg + g;
      const char* src; unsigned dst;
      if (kind == 0)      { src = (const char*)q + tb + row * 256 + 16 * (c ^ row);              dst = C::LDS_Q + rg * 1024; }
      else if (kind == 1) { src = (const char*)kt + tb + row * 256 + 16 * (c ^ row);             dst = C::LDS_KT + rg * 1024; }
      else if (kind == 2) { src = (const char*)vt + tb + row * 256 + 16 * (c ^ (2 * (row & 7))); dst = C::LDS_VT + rg * 1024; }
      else                { src = (const char*)mask + (size_t)b * 512 + (lane & 31) * 16;        dst = C::LDS_MASK; }
      const unsigned m0v = __builtin_amdgcn_readfirstlane(lds0 + dst);
      asm volatile("s_nop 4\n\ts_mov_b32 m0, %0\n\ts_nop 0\n\tglobal_load_lds_dwordx4 %1, off" :: "s"(m0v), "v"(src) : "memory");
    }
    if constexpr (C::TOUCH) {
      const char* src = (const char*)out + tb + lane * 16;
      const unsigned m0v = lds0 + C::LDS_DUMMY;
      asm volatile("s_mov_b32 m0, %0\n\ts_nop 0\n\tglobal_load_lds_dwordx4 %1, off" :: "s"(m0v), "v"(src) : "memory");
    }
  };
  if constexpr (C::PRO == 0) {
    pieces();
#pragma unroll
    for (int s = 0; s < DEPTH; ++s) { issueK(s, s); issueV(s, s); }
  } else {
    issueK(0, 0); issueV(0, 0);
    pieces();
#pragma unroll
    for (int s = 1; s < DEPTH; ++s) { issueK(s, s); issueV(s, s); }
  }
  constexpr int PWAIT = C::PRO == 0 ? DEPTH * 2 * G : (DEPTH - 1) * 2 * G;

  unsigned ka[4], va[8];
#pragma unroll
  for (int kk = 0; kk < 4; ++kk) ka[kk] = c * 256 + 16 * ((g ^ c) ^ (4 * kk));
  {
    const unsigned qq = c >> 2, p = c & 3, row = 4 * g + qq;
#pragma unroll
    for (int dt = 0; dt < 8; ++dt) va[dt] = row * 256 + 32 * (dt ^ (row & 7)) + 8 * p;
  }

  bf16x8 qf[4];
  f32x4 acc[8];
#pragma unroll
  for (int dt = 0; dt < 8; ++dt) acc[dt] = f32x4{0.f, 0.f, 0.f, 0.f};
  float m = -INFINITY, l = 0.f;

  wait_vm<PWAIT>();
  if constexpr (C::STAMP) { unsigned t = (unsigned)__builtin_amdgcn_s_memrealtime(); asm volatile("s_waitcnt lgkmcnt(0)" ::: "memory"); pv = (lane == 17) ? t : pv; pv = (lane == 18) ? (unsigned)t0 : pv; }
  __builtin_amdgcn_s_barrier();
#pragma unroll
  for (int kk = 0; kk < 4; ++kk) qf[kk] = lds_b128(smem + C::LDS_Q + ka[kk]);

  if (w == NW - 1) {
    f32x4 s = {0.f, 0.f, 0.f, 0.f};
#pragma unroll
    for (int kk = 0; kk < 4; ++kk) s = __builtin_amdgcn_mfma_f32_16x16x32_bf16(lds_b128(smem + C::LDS_KT + ka[kk]), qf[kk], s, 0, 0, 0);
    const u32x2 mk = *(const u32x2 LAS*)(smem + C::LDS_MASK + c * 32 + g * 8);
    const float mv[4] = {__uint_as_float(mk[0] << 16), __uint_as_float(mk[0] & 0xffff0000u), __uint_as_float(mk[1] << 16), __uint_as_float(mk[1] & 0xffff0000u)};
    float t[4];
#pragma unroll
    for (int r = 0; r < 4; ++r) t[r] = (s[r] * kScale + (1.0f - mv[r]) * -1000000000.0f) * kL2E;
    const float mx = xg_max(fmaxf(fmaxf(t[0], t[1]), fmaxf(t[2], t[3])));
    m = mx;
    float p[4];
#pragma unroll
    for (int r = 0; r < 4; ++r) { p[r] = __builtin_amdgcn_exp2f(t[r] - mx); l += p[r]; }
    const u32x2 pw = {cvtpk(p[0], p[1]), cvtpk(p[2], p[3])};
    const s16x4 pf = __builtin_bit_cast(s16x4, pw);
#pragma unroll
    for (int dt = 0; dt < 8; ++dt) acc[dt] = __builtin_amdgcn_mfma_f32_16x16x16bf16_1k(lds_tr(smem + C::LDS_VT + va[dt]), pf, acc[dt], 0, 0, 0);
  }

  const char LAS* kring = smem + w * WREG;
  const char LAS* vring = kring + DEPTH * SLOT;

  auto step = [&](int s, auto slot_c, auto wk_c, auto wv_c, auto issue_c) {
    constexpr int slot = decltype(slot_c)::value, WK = decltype(wk_c)::value, WV = decltype(wv_c)::value;
    constexpr bool ISSUE = decltype(issue_c)::value;
    SB();
    wait_vm<WK>();
    if constexpr (C::STAMP) { if ((s % (NS / 16 > 0 ? NS / 16 : 1)) == 0) { unsigned t = (unsigned)__builtin_amdgcn_s_memrealtime(); asm volatile("s_waitcnt lgkmcnt(0)" ::: "memory"); pv = (lane == s / (NS / 16 > 0 ? NS / 16 : 1)) ? t : pv; } }
    unsigned tnow = 0;
    if constexpr (C::PACE && ISSUE) tnow = (unsigned)__builtin_amdgcn_s_memrealtime();
    unsigned dlt = 0;
    if constexpr (C::FOL && ISSUE) dlt = *(volatile unsigned LAS*)(smem + C::LDS_CTL);
    bf16x8 kf[TK][4];
#pragma unroll
    for (int t = 0; t < TK; ++t)
#pragma unroll
      for (int kk = 0; kk < 4; ++kk) kf[t][kk] = lds_b128(kring + slot * SLOT + t * 4096 + ka[kk]);
    if constexpr (ISSUE) {
      wait_lgkm0();
      if constexpr (C::PACE) {
        const unsigned nominal = (unsigned)((s + 1) * C::PACE) / (unsigned)NS;
        unsigned target = nominal;
        if constexpr (C::FOL) target += __builtin_amdgcn_readfirstlane(dlt);
        while (tnow - (unsigned)t0 + phase < target) { __builtin_amdgcn_s_sleep(1); tnow = (unsigned)__builtin_amdgcn_s_memrealtime(); }
        if constexpr (C::FOL) {
          if (lane == 0) { *(volatile unsigned LAS*)(smem + C::LDS_CTL + 16 + 4 * w) = tnow - nominal; if (s == NS - DEPTH - 3) *(volatile unsigned LAS*)(smem + C::LDS_CTL + 4) = 1u; }
        }
      }
      issueK(s + DEPTH, slot);
    }
    SB();
    f32x4 sc[TK];
#pragma unroll
    for (int t = 0; t < TK; ++t) {
      sc[t] = f32x4{0.f, 0.f, 0.f, 0.f};
#pragma unroll
      for (int kk = 0; kk < 4; ++kk) sc[t] = __builtin_amdgcn_mfma_f32_16x16x32_bf16(kf[t][kk], qf[kk], sc[t], 0, 0, 0);
    }
    float mx = fmaxf(fmaxf(sc[0][0], sc[0][1]), fmaxf(sc[0][2], sc[0][3]));
    if constexpr (TK == 2) mx = fmaxf(mx, fmaxf(fmaxf(sc[1][0], sc[1][1]), fmaxf(sc[1][2], sc[1][3])));
    mx = xg_max(mx) * kC;
    float mn;
    if constexpr (C::CRS) {
      if (__builtin_amdgcn_ballot_w64(mx > m) != 0) {
        mn = fmaxf(m, mx);
        const float alpha = __builtin_amdgcn_exp2f(m - mn);
        m = mn;
        l *= alpha;
#pragma unroll
        for (int dt = 0; dt < 8; ++dt) acc[dt] *= alpha;
      }
      mn = m;
    }
    float alpha = 1.f;
    if constexpr (!C::CRS) {
      mn = fmaxf(m, mx);
      alpha = __builtin_amdgcn_exp2f(m - mn);
      m = mn;
    }
    float p[4 * TK];
    float ps = 0.f;
#pragma unroll
    for (int t = 0; t < TK; ++t)
#pragma unroll
      for (int r = 0; r < 4; ++r) { p[4 * t + r] = __builtin_amdgcn_exp2f(fmaf(sc[t][r], kC, -mn)); ps += p[4 * t + r]; }
    if constexpr (C::CRS) l += ps; else l = fmaf(l, alpha, ps);
    if constexpr (!C::CRS) {
#pragma unroll
      for (int dt = 0; dt < 8; ++dt) acc[dt] *= alpha;
    }
    unsigned pw[2 * TK];
#pragma unroll
    for (int i = 0; i < 2 * TK; ++i) pw[i] = cvtpk(p[2 * i], p[2 * i + 1]);
    SB();
    wait_vm<WV>();
    s16x4 vf[8][TK];
#pragma unroll
    for (int dt = 0; dt < 8; ++dt)
#pragma unroll
      for (int t = 0; t < TK; ++t) vf[dt][t] = lds_tr(vring + slot * SLOT + t * 4096 + va[dt]);
    if constexpr (ISSUE) { wait_lgkm0(); issueV(s + DEPTH, slot); }
    SB();
    if constexpr (TK == 2) {
      const u32x4 pq = {pw[0], pw[1], pw[2], pw[3]};
      const bf16x8 pf = __builtin_bit_cast(bf16x8, pq);
#pragma unroll
      for (int dt = 0; dt < 8; ++dt) {
        const s16x8 a = {vf[dt][0][0], vf[dt][0][1], vf[dt][0][2], vf[dt][0][3], vf[dt][1][0], vf[dt][1][1], vf[dt][1][2], vf[dt][1][3]};
        acc[dt] = __builtin_amdgcn_mfma_f32_16x16x32_bf16(__builtin_bit_cast(bf16x8, a), pf, acc[dt], 0, 0, 0);
      }
    } else {
      const u32x2 pq = {pw[0], pw[1]};
      const s16x4 pf = __builtin_bit_cast(s16x4, pq);
#pragma unroll
      for (int dt = 0; dt < 8; ++dt) acc[dt] = __builtin_amdgcn_mfma_f32_16x16x16bf16_1k(vf[dt][0], pf, acc[dt], 0, 0, 0);
    }
  };
  using std::integral_constant;
  constexpr int WS = G * (2 * DEPTH - 1);
  constexpr int R = (NS - DEPTH) / DEPTH;
  for (int r = 0; r < R; ++r)
    sfor<DEPTH>([&](auto i) { step(r * DEPTH + i.value, i, integral_constant<int, WS>{}, integral_constant<int, WS>{}, integral_constant<bool, true>{}); });
  sfor<NS - R * DEPTH>([&](auto i) {
    constexpr int s = R * DEPTH + i.value;
    constexpr int last = (s + DEPTH - 1 < NS - 1) ? s + DEPTH - 1 : NS - 1;
    constexpr bool iss = s + DEPTH < NS;
    step(s, integral_constant<int, s % DEPTH>{}, integral_constant<int, G * (1 + 2 * (last - s))>{},
         integral_constant<int, G * (2 * (last - s) + (iss ? 1 : 0))>{}, integral_constant<bool, iss>{});
  });

  if constexpr (C::STAMP) { unsigned t = (unsigned)__builtin_amdgcn_s_memrealtime(); asm volatile("s_waitcnt lgkmcnt(0)" ::: "memory"); pv = (lane == 16) ? t : pv; }
  l = xg_sum(l);
  {
    char LAS* mr = smem + w * WREG;
#pragma unroll
    for (int dt = 0; dt < 8; ++dt) *(f32x4 LAS*)(mr + c * MROW + (16 * dt + 4 * g) * 4) = acc[dt];
    if (g == 0) { *(float LAS*)(mr + 16 * MROW + c * 8) = m; *(float LAS*)(mr + 16 * MROW + c * 8 + 4) = l; }
  }
  __syncthreads();
  if (tid < 256) {
    const int qi = tid >> 4, dc = tid & 15;
    float mw[NW], lw[NW], M = -INFINITY;
#pragma unroll
    for (int x = 0; x < NW; ++x) {
      mw[x] = *(const float LAS*)(smem + x * WREG + 16 * MROW + qi * 8);
      lw[x] = *(const float LAS*)(smem + x * WREG + 16 * MROW + qi * 8 + 4);
      M = fmaxf(M, mw[x]);
    }
    float L = 0.f;
    f32x4 o0 = {0.f, 0.f, 0.f, 0.f}, o1 = {0.f, 0.f, 0.f, 0.f};
#pragma unroll
    for (int x = 0; x < NW; ++x) {
      const float f = __builtin_amdgcn_exp2f(mw[x] - M);
      L = fmaf(f, lw[x], L);
      const f32x4 a0 = *(const f32x4 LAS*)(smem + x * WREG + qi * MROW + dc * 32);
      const f32x4 a1 = *(const f32x4 LAS*)(smem + x * WREG + qi * MROW + dc * 32 + 16);
      o0 += f * a0; o1 += f * a1;
    }
    const float inv = 1.0f / L;
    const u32x4 ow = {cvtpk(o0[0] * inv, o0[1] * inv), cvtpk(o0[2] * inv, o0[3] * inv), cvtpk(o1[0] * inv, o1[1] * inv), cvtpk(o1[2] * inv, o1[3] * inv)};
    u32x4* op = (u32x4*)(out + ((size_t)bh * T_ + qi) * D_ + dc * 8);
    if constexpr (C::OST == 1) __builtin_nontemporal_store(ow, op);
    else if constexpr (C::OST == 2) asm volatile("global_store_dwordx4 %0, %1, off sc0 sc1\n\ts_nop 1" :: "v"(op), "v"(ow) : "memory");
    else *op = ow;
  }
  if constexpr (C::STAMP) {
    unsigned long long t1 = __builtin_amdgcn_s_memrealtime();
    unsigned xcc; asm volatile("s_getreg_b32 %0, hwreg(HW_REG_XCC_ID)" : "=s"(xcc));
    if (tid == 0) { stamps[2 * bh] = t0; stamps[2 * bh + 1] = t1; stamps[8192 + bh] = xcc & 0xf; }
    if (w == 0 && lane < 32) stamps[16384 + bh * 32 + lane] = pv;
  }
}

}

#define NTHR(C) (C::NTHREADS)

extern "C" void kernel_launch(void* const* d_in, const int* in_sizes, int n_in, void* d_out, int out_size, void* d_ws, size_t ws_size,
                              hipStream_t stream) {
  (void)in_sizes; (void)n_in; (void)out_size; (void)ws_size;
  using C = ta::Cfg<4, 1, 2, 0, 1, 0, 0, 7300, 0, 0, 0, 0, 0, 64, 0, 1024, 60, 0>;
  (void)hipFuncSetAttribute((const void*)ta::tree_attn<C>, hipFuncAttributeMaxDynamicSharedMemorySize, C::LDS_TOTAL);
  hipLaunchKernelGGL((ta::tree_attn<C>), dim3(256), dim3(NTHR(C)), C::LDS_TOTAL, stream,
                     (const uint16_t*)d_in[0], (const uint16_t*)d_in[1], (const uint16_t*)d_in[2], (const uint16_t*)d_in[3],
                     (const uint16_t*)d_in[4], (const uint16_t*)d_in[5], (uint16_t*)d_out, (unsigned long long*)d_ws);
}
```

```cpp
#include <hip/hip_runtime.h>
#include <hip/hip_bf16.h>
#include <stdint.h>
#include <utility>

#define TA_PACE 7200
#define TA_SLACK 20
#define TA_PERIOD 1024
#define TA_GI 2
#define TA_NB 32

namespace {

constexpr int kBH = 8 * 32, kT = 16, kC = 4096, kD = 128;
constexpr int NW = 4;
constexpr int NB = TA_NB;
constexpr int NT = kC / 16 / NW;
constexpr int NSEQ = 2 * NT;
constexpr int R = 4;
constexpr int TILE = 4096;
constexpr int G = 4;
constexpr int LDS_P = R * TILE;
constexpr int LDS_A = LDS_P + NB * 512;
constexpr int WREG = LDS_A + (NB / 4) * 256;
constexpr int MROW = 528;
constexpr int LDS_Q = NW * WREG, LDS_KT = LDS_Q + TILE, LDS_VT = LDS_KT + TILE, LDS_MASK = LDS_VT + TILE;
constexpr int LDS_CTL = LDS_MASK + 1024;
constexpr int LDS_DUMMY = LDS_CTL + 64;
constexpr int LDS_USED = LDS_DUMMY + 256;
constexpr int LDS_TOTAL = LDS_USED > 83968 ? LDS_USED : 83968;
static_assert(16 * MROW + 128 <= R * TILE && LDS_TOTAL <= 163840 && NB % 4 == 0 && NT % NB == 0, "layout");

typedef __attribute__((ext_vector_type(8))) __bf16 bf16x8;
typedef __attribute__((ext_vector_type(4))) short s16x4;
typedef __attribute__((ext_vector_type(4))) float f32x4;
typedef __attribute__((ext_vector_type(2))) float f32x2;
typedef __attribute__((ext_vector_type(4))) unsigned u32x4;
typedef __attribute__((ext_vector_type(2))) unsigned u32x2;
#define LAS __attribute__((address_space(3)))
#define SB() __builtin_amdgcn_sched_barrier(0)

constexpr float kScale = 0.088388347648318440f;
constexpr float kL2E = 1.4426950408889634f;
constexpr float kSL = kScale * kL2E;

__device__ __forceinline__ unsigned cvtpk(float lo, float hi) {
  typedef __attribute__((ext_vector_type(2))) __bf16 bf16x2;
  const f32x2 v = {lo, hi};
  return __builtin_bit_cast(unsigned, __builtin_convertvector(v, bf16x2));
}
__device__ __forceinline__ float xg_max(float x) {
  auto s = __builtin_amdgcn_permlane16_swap(__float_as_uint(x), __float_as_uint(x), false, false);
  x = fmaxf(__uint_as_float(s[0]), __uint_as_float(s[1]));
  auto t = __builtin_amdgcn_permlane32_swap(__float_as_uint(x), __float_as_uint(x), false, false);
  return fmaxf(__uint_as_float(t[0]), __uint_as_float(t[1]));
}
__device__ __forceinline__ float xg_sum(float x) {
  auto s = __builtin_amdgcn_permlane16_swap(__float_as_uint(x), __float_as_uint(x), false, false);
  x = __uint_as_float(s[0]) + __uint_as_float(s[1]);
  auto t = __builtin_amdgcn_permlane32_swap(__float_as_uint(x), __float_as_uint(x), false, false);
  return __uint_as_float(t[0]) + __uint_as_float(t[1]);
}

__device__ __forceinline__ void dma_tile(unsigned m0v, unsigned v0, unsigned v1, unsigned v2, unsigned v3,
                                         __amdgpu_buffer_rsrc_t srd, unsigned soff) {
  asm volatile(
      "s_mov_b32 m0, %0\n\t"
      "s_nop 0\n\t"
      "buffer_load_dwordx4 %1, %5, %6 offen nt lds\n\t"
      "buffer_load_dwordx4 %2, %5, %6 offen offset:1024 nt lds\n\t"
      "buffer_load_dwordx4 %3, %5, %6 offen offset:2048 nt lds\n\t"
      "buffer_load_dwordx4 %4, %5, %6 offen offset:3072 nt lds"
      :: "s"(m0v), "v"(v0), "v"(v1), "v"(v2), "v"(v3), "s"(srd), "s"(soff) : "memory");
}
__device__ __forceinline__ void dma_piece(unsigned m0v, const void* src) {
  asm volatile("s_nop 4\n\ts_mov_b32 m0, %0\n\ts_nop 0\n\tglobal_load_lds_dwordx4 %1, off" :: "s"(m0v), "v"(src) : "memory");
}
template <int N> __device__ __forceinline__ void wait_vm() {
  static_assert(N >= 0 && N <= 63, "vmcnt");
  asm volatile("s_waitcnt vmcnt(%0)" :: "i"(N) : "memory");
}
__device__ __forceinline__ void wait_lgkm0() { asm volatile("s_waitcnt lgkmcnt(0)" ::: "memory"); }
__device__ __forceinline__ unsigned clock10ns() { return (unsigned)__builtin_amdgcn_s_memrealtime(); }
__device__ __forceinline__ bf16x8 lds_b128(const char LAS* p) { return *(const bf16x8 LAS*)p; }
__device__ __forceinline__ s16x4 lds_tr(const char LAS* p) { return __builtin_amdgcn_ds_read_tr16_b64_v4i16((s16x4 LAS*)p); }

template <class F, int... I> __device__ __forceinline__ void sfor_impl(F&& f, std::integer_sequence<int, I...>) { (f(std::integral_constant<int, I>{}), ...); }
template <int N, class F> __device__ __forceinline__ void sfor(F&& f) { sfor_impl(f, std::make_integer_sequence<int, N>{}); }

}

__global__ __launch_bounds__((NW + 1) * 64, 2) void tree_attn_kernel(
    const uint16_t* __restrict__ q, const uint16_t* __restrict__ kc, const uint16_t* __restrict__ vc,
    const uint16_t* __restrict__ kt, const uint16_t* __restrict__ vt, const uint16_t* __restrict__ mask,
    uint16_t* __restrict__ out, unsigned long long* __restrict__ ws) {
  extern __shared__ __attribute__((aligned(16))) char smem_[];
  char LAS* smem = (char LAS*)smem_;
  unsigned long long tstart;
  asm volatile("s_memrealtime %0" : "=s"(tstart));
  const int tid = threadIdx.x, lane = tid & 63;
  const int w = __builtin_amdgcn_readfirstlane(tid >> 6);
  const int bh = blockIdx.x;
  const unsigned g = lane >> 4, c = lane & 15;
  const unsigned lds0 = (unsigned)(size_t)smem;

  if (w == NW) {
    typedef __attribute__((address_space(1))) unsigned long long gu64;
    gu64* line = (gu64*)ws + (bh >> 3) * 32;
    volatile unsigned LAS* ctl = (volatile unsigned LAS*)(smem + LDS_CTL);
    asm volatile("s_waitcnt lgkmcnt(0)" : "+s"(tstart));
    const unsigned T0 = (unsigned)tstart;
    if (lane < 8) ctl[lane] = lane >= 4 ? T0 : 0u;
    wait_lgkm0();
    __builtin_amdgcn_s_barrier();
    for (unsigned tb = ((T0 + 300u) | (TA_PERIOD - 1)) + 1u;; tb += TA_PERIOD) {
      unsigned stop = ctl[1];
      while ((int)(clock10ns() - tb) < 0 && !stop) { __builtin_amdgcn_s_sleep(8); stop = ctl[1]; }
      if (__builtin_amdgcn_readfirstlane(stop)) break;
      int e = (int)(ctl[4 + (lane & 3)] - T0);
      e = max(e, __shfl_xor(e, 1));
      e = max(e, __shfl_xor(e, 2));
      if (lane == 0) __hip_atomic_store(line + (bh & 7), ((unsigned long long)tb << 32) | (unsigned)(T0 + e), __ATOMIC_RELAXED, __HIP_MEMORY_SCOPE_AGENT);
      while ((int)(clock10ns() - (tb + TA_PERIOD / 2)) < 0 && !stop) { __builtin_amdgcn_s_sleep(8); stop = ctl[1]; }
      if (__builtin_amdgcn_readfirstlane(stop)) break;
      const unsigned long long v = __hip_atomic_load(line + (lane & 7), __ATOMIC_RELAXED, __HIP_MEMORY_SCOPE_AGENT);
      const bool mine = ((unsigned)(v >> 32) - (T0 - 300u)) <= (tb - (T0 - 300u));
      int d = mine ? (int)((unsigned)v - T0) : 0;
      d = min(max(d, -2000), 2000);
      int n = mine ? 1 : 0;
      d += __shfl_xor(d, 1); d += __shfl_xor(d, 2); d += __shfl_xor(d, 4);
      n += __shfl_xor(n, 1); n += __shfl_xor(n, 2); n += __shfl_xor(n, 4);
      d = (int)((float)d / (float)max(n, 1));
      if (lane == 0) ctl[0] = (unsigned)min(max(d - TA_SLACK, 0), 1500);
    }
    return;
  }

  const uint16_t* kh = kc + (size_t)bh * kC * kD;
  const uint16_t* vh = vc + (size_t)bh * kC * kD;
  const __amdgpu_buffer_rsrc_t srdK = __builtin_amdgcn_make_buffer_rsrc((void*)kh, 0, kC * kD * 2, 0x00020000);

  unsigned vk[4], vv[2];
#pragma unroll
  for (int i = 0; i < 4; ++i) vk[i] = g * 256 + 16 * ((c ^ g) ^ (4 * i));
#pragma unroll
  for (int j = 0; j < 2; ++j) vv[j] = g * 256 + 16 * (c ^ (2 * g) ^ (8 * j));

  const unsigned ring = lds0 + w * WREG;
  auto tile_off = [&](int t) -> unsigned { return (unsigned)(((t / TA_GI) * NW + w) * TA_GI + t % TA_GI) * TILE; };
  auto issueK = [&](int t, int slot) { dma_tile(ring + slot * TILE, vk[0], vk[1], vk[2], vk[3], srdK, tile_off(t)); };

  issueK(0, 0);
  {
    const size_t tb = (size_t)bh * kT * kD * 2;
    const unsigned row = 4 * w + g;
    dma_piece(lds0 + LDS_Q + w * 1024, (const char*)q + tb + row * 256 + 16 * (c ^ row));
    dma_piece(lds0 + LDS_KT + w * 1024, (const char*)kt + tb + row * 256 + 16 * (c ^ row));
    dma_piece(lds0 + LDS_VT + w * 1024, (const char*)vt + tb + row * 256 + 16 * (c ^ (2 * (row & 7))));
    dma_piece(lds0 + LDS_MASK, (const char*)mask + (size_t)(bh >> 5) * 512 + (lane & 31) * 16);
  }
#pragma unroll
  for (int j = 1; j < R; ++j) issueK(j, j);
  asm volatile("s_mov_b32 m0, %0\n\ts_nop 0\n\tbuffer_load_dword %1, %2, 0 offen nt lds"
               :: "s"(lds0 + LDS_DUMMY), "v"(0u), "s"(__builtin_amdgcn_make_buffer_rsrc((void*)vh, 0, kC * kD * 2, 0x00020000)) : "memory");
  asm volatile("s_waitcnt lgkmcnt(0)" : "+s"(tstart));
  const unsigned t0 = (unsigned)tstart;

  unsigned ka[4], va[8];
#pragma unroll
  for (int kk = 0; kk < 4; ++kk) ka[kk] = c * 256 + 16 * ((g ^ c) ^ (4 * kk));
  {
    const unsigned row = 4 * g + (c >> 2), p = c & 3;
#pragma unroll
    for (int dt = 0; dt < 8; ++dt) va[dt] = row * 256 + 32 * (dt ^ (row & 7)) + 8 * p;
  }

  bf16x8 qf[4];
  f32x4 acc[8];
#pragma unroll
  for (int dt = 0; dt < 8; ++dt) acc[dt] = f32x4{0.f, 0.f, 0.f, 0.f};
  float m = -INFINITY, l = 0.f;

  wait_vm<G * (R - 1)>();
  __builtin_amdgcn_s_barrier();
#pragma unroll
  for (int kk = 0; kk < 4; ++kk) qf[kk] = lds_b128(smem + LDS_Q + ka[kk]);

  if (w == NW - 1) {
    f32x4 s = {0.f, 0.f, 0.f, 0.f};
#pragma unroll
    for (int kk = 0; kk < 4; ++kk) s = __builtin_amdgcn_mfma_f32_16x16x32_bf16(lds_b128(smem + LDS_KT + ka[kk]), qf[kk], s, 0, 0, 0);
    const u32x2 mk = *(const u32x2 LAS*)(smem + LDS_MASK + c * 32 + g * 8);
    const float mv[4] = {__uint_as_float(mk[0] << 16), __uint_as_float(mk[0] & 0xffff0000u),
                         __uint_as_float(mk[1] << 16), __uint_as_float(mk[1] & 0xffff0000u)};
    float t[4];
#pragma unroll
    for (int r = 0; r < 4; ++r) t[r] = (s[r] * kScale + (1.0f - mv[r]) * -1000000000.0f) * kL2E;
    m = xg_max(fmaxf(fmaxf(t[0], t[1]), fmaxf(t[2], t[3])));
    float p[4];
#pragma unroll
    for (int r = 0; r < 4; ++r) { p[r] = __builtin_amdgcn_exp2f(t[r] - m); l += p[r]; }
    const u32x2 pw = {cvtpk(p[0], p[1]), cvtpk(p[2], p[3])};
#pragma unroll
    for (int dt = 0; dt < 8; ++dt)
      acc[dt] = __builtin_amdgcn_mfma_f32_16x16x16bf16_1k(lds_tr(smem + LDS_VT + va[dt]), __builtin_bit_cast(s16x4, pw), acc[dt], 0, 0, 0);
  }

  const char LAS* wl = smem + w * WREG;
  char LAS* pl = smem + w * WREG + LDS_P + lane * 8;
  char LAS* al = smem + w * WREG + LDS_A + lane * 4;

  struct Next { __amdgpu_buffer_rsrc_t srd; unsigned vo[4]; unsigned off; };
  auto next_of = [&](int jn) {
    const int blk = jn / (2 * NB), r = jn % (2 * NB);
    const bool isV = r >= NB;
    Next n;
    n.srd = __builtin_amdgcn_make_buffer_rsrc((void*)(isV ? vh : kh), 0, kC * kD * 2, 0x00020000);
#pragma unroll
    for (int i = 0; i < 4; ++i) n.vo[i] = isV ? vv[i & 1] : vk[i];
    n.off = tile_off(blk * NB + (isV ? r - NB : r));
    return n;
  };
  auto refill = [&](int j, int slot, const Next& n, unsigned long long tn64, unsigned shift) {
    asm volatile("s_waitcnt lgkmcnt(0)" : "+s"(tn64) :: "memory");
    unsigned tnow = (unsigned)tn64;
    const unsigned nominal = (unsigned)((j + 1) * TA_PACE) / (unsigned)NSEQ;
    const unsigned target = nominal + __builtin_amdgcn_readfirstlane(shift);
    while (tnow - t0 < target) { __builtin_amdgcn_s_sleep(1); tnow = clock10ns(); }
    dma_tile(ring + slot * TILE, n.vo[0], n.vo[1], n.vo[2], n.vo[3], n.srd, n.off + (unsigned)(((slot / TA_GI) * NW * TA_GI + slot % TA_GI) * TILE));
    if (lane == 0) {
      *(volatile unsigned LAS*)(smem + LDS_CTL + 16 + 4 * w) = tnow - nominal;
      if (j == NSEQ - R - 7) *(volatile unsigned LAS*)(smem + LDS_CTL + 4) = 1u;
    }
  };
  auto ksub = [&](int j0, int sb) {
    f32x4 sc[4];
    const Next n = next_of(j0 + R);
    sfor<4>([&](auto ic) {
      constexpr int i = decltype(ic)::value;
      SB();
      wait_vm<G * (R - 1)>();
      unsigned long long tn64;
      asm volatile("s_memrealtime %0" : "=s"(tn64));
      const unsigned shift = *(volatile unsigned LAS*)(smem + LDS_CTL);
      bf16x8 kf[4];
#pragma unroll
      for (int kk = 0; kk < 4; ++kk) kf[kk] = lds_b128(wl + i * TILE + ka[kk]);
      refill(j0 + i, i, n, tn64, shift);
      SB();
      sc[i] = f32x4{0.f, 0.f, 0.f, 0.f};
#pragma unroll
      for (int kk = 0; kk < 4; ++kk) sc[i] = __builtin_amdgcn_mfma_f32_16x16x32_bf16(kf[kk], qf[kk], sc[i], 0, 0, 0);
    });
    float mx = -INFINITY;
#pragma unroll
    for (int i = 0; i < 4; ++i) mx = fmaxf(mx, fmaxf(fmaxf(sc[i][0], sc[i][1]), fmaxf(sc[i][2], sc[i][3])));
    mx = xg_max(mx) * kSL;
    const float mn = fmaxf(m, mx);
    const float alpha = __builtin_amdgcn_exp2f(m - mn);
    m = mn;
    float ps = 0.f;
#pragma unroll
    for (int i = 0; i < 4; ++i) {
      float p[4];
#pragma unroll
      for (int r = 0; r < 4; ++r) p[r] = __builtin_amdgcn_exp2f(fmaf(sc[i][r], kSL, -mn));
      ps += (p[0] + p[1]) + (p[2] + p[3]);
      *(u32x2 LAS*)(pl + (sb * 4 + i) * 512) = u32x2{cvtpk(p[0], p[1]), cvtpk(p[2], p[3])};
    }
    l = fmaf(l, alpha, ps);
    *(float LAS*)(al + sb * 256) = alpha;
  };
  auto vsub = [&](int j0, int sb, auto last_c) {
    constexpr bool LAST = decltype(last_c)::value;
    const float alpha = *(const float LAS*)(al + sb * 256);
#pragma unroll
    for (int dt = 0; dt < 8; ++dt) acc[dt] *= alpha;
    const Next n = next_of(LAST ? 0 : j0 + R);
    sfor<4>([&](auto ic) {
      constexpr int i = decltype(ic)::value;
      SB();
      wait_vm<LAST ? G * (R - 1 - i) : G * (R - 1)>();
      unsigned long long tn64 = 0;
      unsigned shift = 0;
      if constexpr (!LAST) {
        asm volatile("s_memrealtime %0" : "=s"(tn64));
        shift = *(volatile unsigned LAS*)(smem + LDS_CTL);
      }
      const s16x4 pf = *(const s16x4 LAS*)(pl + (sb * 4 + i) * 512);
      s16x4 vf[8];
#pragma unroll
      for (int dt = 0; dt < 8; ++dt) vf[dt] = lds_tr(wl + i * TILE + va[dt]);
      if constexpr (!LAST) refill(j0 + i, i, n, tn64, shift);
      SB();
#pragma unroll
      for (int dt = 0; dt < 8; ++dt) acc[dt] = __builtin_amdgcn_mfma_f32_16x16x16bf16_1k(vf[dt], pf, acc[dt], 0, 0, 0);
    });
  };
  using std::integral_constant;
  for (int blk = 0; blk < NT / NB; ++blk) {
    for (int sb = 0; sb < NB / 4; ++sb) ksub(blk * 2 * NB + 4 * sb, sb);
    const int nv = NB / 4 - (blk == NT / NB - 1 ? 1 : 0);
    for (int sb = 0; sb < nv; ++sb) vsub(blk * 2 * NB + NB + 4 * sb, sb, integral_constant<bool, false>{});
  }
  vsub(NSEQ - 4, NB / 4 - 1, integral_constant<bool, true>{});

  l = xg_sum(l);
  {
    char LAS* mr = smem + w * WREG;
#pragma unroll
    for (int dt = 0; dt < 8; ++dt) *(f32x4 LAS*)(mr + c * MROW + (16 * dt + 4 * g) * 4) = acc[dt];
    if (g == 0) *(f32x2 LAS*)(mr + 16 * MROW + c * 8) = f32x2{m, l};
  }
  __syncthreads();
  {
    const int qi = tid >> 4, dc = tid & 15;
    float mw[NW], lw[NW], M = -INFINITY;
#pragma unroll
    for (int x = 0; x < NW; ++x) {
      mw[x] = *(const float LAS*)(smem + x * WREG + 16 * MROW + qi * 8);
      lw[x] = *(const float LAS*)(smem + x * WREG + 16 * MROW + qi * 8 + 4);
      M = fmaxf(M, mw[x]);
    }
    float L = 0.f;
    f32x4 o0 = {0.f, 0.f, 0.f, 0.f}, o1 = {0.f, 0.f, 0.f, 0.f};
#pragma unroll
    for (int x = 0; x < NW; ++x) {
      const float f = __builtin_amdgcn_exp2f(mw[x] - M);
      L = fmaf(f, lw[x], L);
      o0 += f * *(const f32x4 LAS*)(smem + x * WREG + qi * MROW + dc * 32);
      o1 += f * *(const f32x4 LAS*)(smem + x * WREG + qi * MROW + dc * 32 + 16);
    }
    const float inv = __builtin_amdgcn_rcpf(L);
    const u32x4 ow = {cvtpk(o0[0] * inv, o0[1] * inv), cvtpk(o0[2] * inv, o0[3] * inv),
                      cvtpk(o1[0] * inv, o1[1] * inv), cvtpk(o1[2] * inv, o1[3] * inv)};
    *(u32x4*)(out + ((size_t)bh * kT + qi) * kD + dc * 8) = ow;
  }
}

extern "C" void kernel_launch(void* const* d_in, const int* in_sizes, int n_in, void* d_out, int out_size, void* d_ws, size_t ws_size,
                              hipStream_t stream) {
  (void)in_sizes; (void)n_in; (void)out_size; (void)ws_size;
  (void)hipFuncSetAttribute((const void*)tree_attn_kernel, hipFuncAttributeMaxDynamicSharedMemorySize, LDS_TOTAL);
  hipLaunchKernelGGL(tree_attn_kernel, dim3(kBH), dim3((NW + 1) * 64), LDS_TOTAL, stream,
                     (const uint16_t*)d_in[0], (const uint16_t*)d_in[1], (const uint16_t*)d_in[2], (const uint16_t*)d_in[3],
                     (const uint16_t*)d_in[4], (const uint16_t*)d_in[5], (uint16_t*)d_out, (unsigned long long*)d_ws);
}
```
